```python
import torch

def _softmax(x, axis=-1):
    return torch.softmax(x, dim=axis)
def _gelu(x):
    return torch.nn.functional.gelu(x, approximate="tanh")
def _silu(x):
    return torch.nn.functional.silu(x)
def _sigmoid(x):
    return torch.sigmoid(x)
def _layernorm(x, axis=-1, eps=1e-5):
    m = x.mean(dim=axis, keepdim=True)
    v = x.var(dim=axis, keepdim=True, unbiased=False)
    return (x - m) / torch.sqrt(v + eps)
def _scatter_add(tgt, idx, src, axis=0):
    return tgt.clone().index_add_(axis, idx, src)
def _segment_sum(src, seg, n):
    out = torch.zeros((n,) + src.shape[1:], dtype=src.dtype)
    return out.index_add_(0, seg, src)
def _pad(x, pw):
    flat = tuple(v for pair in reversed(pw) for v in pair)
    return torch.nn.functional.pad(x, flat)

kernel_name = 'gated_mix__linear_kernel_hedgehog__glu_gated_geglu__prompt'

def setup_inputs():

    _w = (lambda a: (lambda t: t.to(torch.bfloat16) if t.is_floating_point() else t)(torch.as_tensor(a)))
    def _A_setup():
        B, S, D, H = 1, 2048, 1024, 32
        Dh = D // H
        return {
            "x": _w((lambda *s: torch.randn(*s, dtype=torch.bfloat16) * 0.05)(B, S, D)),
            "wqkv": _w((lambda *s: torch.randn(*s, dtype=torch.bfloat16) * 0.8)(D, 3 * D)),
            "w_phi": _w((lambda *s: torch.randn(*s, dtype=torch.bfloat16) * 0.8)(H, Dh, Dh) * (Dh ** -0.5)),
            "wo": _w((lambda *s: torch.randn(*s, dtype=torch.bfloat16) * 0.05)(D, D)),
        }
    _A_in = _A_setup()

    def _B_setup():
        B, S, Dm, Df = 1, 2048, 1024, 3584
        return {
            "x": _w((lambda *s: torch.randn(*s, dtype=torch.bfloat16) * 0.05)(B, S, Dm)),
            "w_gate": _w((lambda *s: torch.randn(*s, dtype=torch.bfloat16) * 1.6)(Dm, Df) * (Dm ** -0.5)),
            "w_up": _w((lambda *s: torch.randn(*s, dtype=torch.bfloat16) * 1.6)(Dm, Df) * (Dm ** -0.5)),
            "w_down": _w((lambda *s: torch.randn(*s, dtype=torch.bfloat16) * 1.6)(Df, Dm) * (Df ** -0.5)),
        }
    _B_in = _B_setup()

    return {
        "A_x": _A_in["x"],
        "A_wqkv": _A_in["wqkv"],
        "A_w_phi": _A_in["w_phi"],
        "A_wo": _A_in["wo"],
        "B_x": _B_in["x"],
        "B_w_gate": _B_in["w_gate"],
        "B_w_up": _B_in["w_up"],
        "B_w_down": _B_in["w_down"],
        "gate_w": _w((lambda *s: torch.randn(*s, dtype=torch.bfloat16) * 0.05)(1024) * 16.0),
    }

def reference(A_x, A_wqkv, A_w_phi, A_wo, B_x, B_w_gate, B_w_up, B_w_down, gate_w):
    def _A_ref(x, wqkv, w_phi, wo):
        B, S, D = x.shape
        H = 32; Dh = D // H
        qkv = (lambda x, *d: x.permute(*d))((x @ wqkv).reshape(B, S, 3, H, Dh), 2, 0, 3, 1, 4)
        q, k, v = qkv[0], qkv[1], qkv[2]
        pq = torch.einsum("bhsd,hdf->bhsf", q, w_phi)
        pk = torch.einsum("bhsd,hdf->bhsf", k, w_phi)
        phi_q = torch.concatenate([_softmax(pq, axis=-1), _softmax(-pq, axis=-1)], axis=-1)
        phi_k = torch.concatenate([_softmax(pk, axis=-1), _softmax(-pk, axis=-1)], axis=-1)
        kv = torch.einsum("bhsf,bhsd->bhfd", phi_k, v)
        z  = torch.einsum("bhsf->bhf", phi_k)
        num = torch.einsum("bhqf,bhfd->bhqd", phi_q, kv)
        den = torch.einsum("bhqf,bhf->bhq", phi_q, z)[..., None] + 1e-6
        o = num / den
        return (lambda x, *d: x.permute(*d))(o, 0, 2, 1, 3).reshape(B, S, D) @ wo

    def _B_ref(x, w_gate, w_up, w_down):
        g = torch.einsum("bsd,df->bsf", x, w_gate)
        u = torch.einsum("bsd,df->bsf", x, w_up)
        h = _gelu(g) * u
        return torch.einsum("bsf,fd->bsd", h, w_down)

    x = A_x
    g = _sigmoid((x * gate_w).sum(-1, keepdims=True))
    a = _A_ref(x, A_wqkv, A_w_phi, A_wo)
    b = _B_ref(x, B_w_gate, B_w_up, B_w_down)
    return g * a + (1 - g) * b
```

```cpp
#include <hip/hip_runtime.h>
#include <hip/hip_bf16.h>
#include <stdint.h>

typedef short bf16x8 __attribute__((ext_vector_type(8)));
typedef short s16x4 __attribute__((ext_vector_type(4)));
typedef float f32x4 __attribute__((ext_vector_type(4)));
typedef float f32x2 __attribute__((ext_vector_type(2)));
typedef unsigned u32x4 __attribute__((ext_vector_type(4)));
typedef unsigned u32x2 __attribute__((ext_vector_type(2)));
typedef __bf16 bf16x2_t __attribute__((ext_vector_type(2)));
#define LAS __attribute__((address_space(3)))
#define GAS __attribute__((address_space(1)))
#define DI __device__ __forceinline__

constexpr int S_ = 2048, D_ = 1024, F_ = 3584;
constexpr int KVP = 2112;

DI void glds_s(unsigned voff, const void* sbase, unsigned ldsaddr) {
  asm volatile("s_mov_b32 m0, %2\n\ts_nop 0\n\tglobal_load_lds_dwordx4 %0, %1" ::"v"(voff), "s"(sbase), "s"(ldsaddr) : "memory");
}
DI void glds_v(const void* vaddr, unsigned ldsaddr) {
  asm volatile("s_mov_b32 m0, %1\n\ts_nop 0\n\tglobal_load_lds_dwordx4 %0, off" ::"v"(vaddr), "s"(ldsaddr) : "memory");
}
DI const char* uni_ptr(const char* p) {
  const unsigned long long v = (unsigned long long)p;
  const unsigned lo = __builtin_amdgcn_readfirstlane((unsigned)v), hi = __builtin_amdgcn_readfirstlane((unsigned)(v >> 32));
  return (const char*)(((unsigned long long)hi << 32) | lo);
}
#define WAIT_V(n) asm volatile("s_waitcnt vmcnt(" #n ")" ::: "memory")
#define WAIT_L0() __builtin_amdgcn_s_waitcnt(0xC07F)
#define SCHED() __builtin_amdgcn_sched_barrier(0)
#define BAR() do { SCHED(); asm volatile("" ::: "memory"); __builtin_amdgcn_s_barrier(); asm volatile("" ::: "memory"); SCHED(); } while (0)
#define MFMA_ACC(c, a, b) asm volatile("v_mfma_f32_16x16x32_bf16 %0, %1, %2, %0" : "+v"(c) : "v"(a), "v"(b))
#define MFMA_DRAIN() do { SCHED(); asm volatile("s_nop 7\n\ts_nop 7" ::: "memory"); SCHED(); } while (0)
#define MFMA(a, b, c) __builtin_amdgcn_mfma_f32_16x16x32_bf16((a), (b), (c), 0, 0, 0)

DI unsigned pk2(float lo, float hi) {
  f32x2 v = {lo, hi};
  bf16x2_t r = __builtin_convertvector(v, bf16x2_t);
  return __builtin_bit_cast(unsigned, r);
}
DI bf16x8 pack8(const f32x4& a, const f32x4& b) {
  u32x4 p = {pk2(a[0], a[1]), pk2(a[2], a[3]), pk2(b[0], b[1]), pk2(b[2], b[3])};
  return __builtin_bit_cast(bf16x8, p);
}
DI bf16x8 tr_frag(LAS char* p, int off) {
  s16x4 a = __builtin_amdgcn_ds_read_tr16_b64_v4i16((LAS s16x4*)(p + off));
  s16x4 b = __builtin_amdgcn_ds_read_tr16_b64_v4i16((LAS s16x4*)(p + off + 512));
  return __builtin_shufflevector(a, b, 0, 1, 2, 3, 4, 5, 6, 7);
}
DI float bf2f(unsigned short u) { return __uint_as_float(((unsigned)u) << 16); }
DI float gelu_mul(float x, float u) {
  const float c0 = -2.3022081984f;
  const float c1 = -2.3022081984f * 0.044715f;
  float t = x * x;
  float p = __builtin_fmaf(t, c1, c0);
  float e = __builtin_amdgcn_exp2f(x * p);
  float r = __builtin_amdgcn_rcpf(1.0f + e);
  return x * r * u;
}
DI float xrow_max(float v) { v = fmaxf(v, __shfl_xor(v, 16)); return fmaxf(v, __shfl_xor(v, 32)); }
DI float xrow_min(float v) { v = fminf(v, __shfl_xor(v, 16)); return fminf(v, __shfl_xor(v, 32)); }
DI float xrow_sum(float v) { v += __shfl_xor(v, 16); return v + __shfl_xor(v, 32); }

DI void feature_map(const f32x4& p0, const f32x4& p1, bf16x8& fpos, bf16x8& fneg) {
  const float L2E = 1.4426950408889634f;
  float mx = fmaxf(fmaxf(fmaxf(p0[0], p0[1]), fmaxf(p0[2], p0[3])), fmaxf(fmaxf(p1[0], p1[1]), fmaxf(p1[2], p1[3])));
  float mn = fminf(fminf(fminf(p0[0], p0[1]), fminf(p0[2], p0[3])), fminf(fminf(p1[0], p1[1]), fminf(p1[2], p1[3])));
  mx = xrow_max(mx); mn = xrow_min(mn);
  const float a = -mx * L2E, b = mn * L2E;
  f32x4 e0, e1, n0, n1;
#pragma unroll
  for (int r = 0; r < 4; ++r) {
    e0[r] = __builtin_amdgcn_exp2f(__builtin_fmaf(p0[r], L2E, a));
    e1[r] = __builtin_amdgcn_exp2f(__builtin_fmaf(p1[r], L2E, a));
    n0[r] = __builtin_amdgcn_exp2f(__builtin_fmaf(p0[r], -L2E, b));
    n1[r] = __builtin_amdgcn_exp2f(__builtin_fmaf(p1[r], -L2E, b));
  }
  float sp = ((e0[0] + e0[1]) + (e0[2] + e0[3])) + ((e1[0] + e1[1]) + (e1[2] + e1[3]));
  float sn = ((n0[0] + n0[1]) + (n0[2] + n0[3])) + ((n1[0] + n1[1]) + (n1[2] + n1[3]));
  sp = xrow_sum(sp); sn = xrow_sum(sn);
  const float rp = __builtin_amdgcn_rcpf(sp), rn = __builtin_amdgcn_rcpf(sn);
  e0 *= rp; e1 *= rp; n0 *= rn; n1 *= rn;
  fpos = pack8(e0, e1); fneg = pack8(n0, n1);
}

constexpr int K1_A0 = 0, K1_ABUF = 32768, K1_B0 = 65536, K1_BBUF = 40960, K1_WPHI = 147456, K1_LDS = 147456 + 8192;

constexpr unsigned long long k1_tab(int var, int wn) {
  unsigned long long t = 0;
  for (int i = 0; i < 10; ++i) {
    const int s = i >> 1, half = i & 1;
    int panel = 0, c32 = 0;
    if (var <= 1) {
      const int b = 5 * wn + s;
      if (var == 1) { if (b < 2) { panel = 4; c32 = b + 2 * half; } else { panel = ((b - 2) >> 2) + 2 * half; c32 = (b - 2) & 3; } }
      else          { if (b >= 8) { panel = 4; c32 = b - 8 + 2 * half; } else { panel = (b >> 2) + 2 * half; c32 = b & 3; } }
    } else if (var == 2) {
      if (s < 2) { panel = half; c32 = 2 * wn + s; } else { const int u = 3 * wn + s - 2; panel = 2 + (u >> 1); c32 = 2 * (u & 1) + half; }
    } else if (var == 3) {
      const int u = 5 * wn + s; panel = u >> 1; c32 = 2 * (u & 1) + half;
    } else {
      if (s == 4) { panel = 4; c32 = 2 * wn + half; } else { panel = (s & 1) * 2 + wn; c32 = (s >> 1) * 2 + half; }
    }
    t |= (unsigned long long)(panel * 4 + c32) << (5 * i);
  }
  return t;
}

struct K1Ctx {
  const char* sA;
  const char* sB[4];
  unsigned stepB[4];
  const char* vB4;
  unsigned stepB4;
  unsigned voffA0, voffA1, voffB[4];
  unsigned ldsA, ldsB;
};

template <int BUF> DI void k1_issue_A(K1Ctx& c) {
#pragma unroll
  for (int i = 0; i < 4; ++i) glds_s((i & 1) ? c.voffA1 : c.voffA0, c.sA + i * 16384, c.ldsA + BUF * K1_ABUF + i * 1024);
  c.sA += 128;
}
template <int BUF> DI void k1_issue_B(K1Ctx& c) {
#pragma unroll
  for (int i = 0; i < 4; ++i) { glds_s(c.voffB[i], c.sB[i], c.ldsB + BUF * K1_BBUF + i * 8192); c.sB[i] += c.stepB[i]; }
  glds_v(c.vB4, c.ldsB + BUF * K1_BBUF + 4 * 8192); c.vB4 += c.stepB4;
}

template <int BUF, int KS> DI void k1_load(LAS char* lds, const unsigned (&aB)[10], const unsigned (&aA)[2], bf16x8 (&fb)[10], bf16x8 (&fa)[4]) {
#pragma unroll
  for (int m = 0; m < 4; ++m) fa[m] = *(LAS bf16x8*)(lds + aA[KS] + (K1_A0 + BUF * K1_ABUF + m * 2048));
#pragma unroll
  for (int i = 0; i < 10; ++i) fb[i] = tr_frag(lds + aB[i], BUF * K1_BBUF + KS * 4096);
}
DI void k1_mma(f32x4 (&acc)[10][4], const bf16x8 (&fb)[10], const bf16x8 (&fa)[4]) {
  __builtin_amdgcn_s_setprio(1);
#pragma unroll
  for (int i = 0; i < 10; ++i)
#pragma unroll
    for (int m = 0; m < 4; ++m) MFMA_ACC(acc[i][m], fb[i], fa[m]);
  __builtin_amdgcn_s_setprio(0);
}
template <int GRP, int BUF, int MODE>
DI void k1_tile(LAS char* lds, K1Ctx& c, const unsigned (&aB)[10], const unsigned (&aA)[2], f32x4 (&acc)[10][4]) {
  bf16x8 fb[10], fa[4];
  k1_load<BUF, 0>(lds, aB, aA, fb, fa);
  if (MODE < 2) { if (GRP == 0) k1_issue_A<BUF ^ 1>(c); else k1_issue_B<BUF ^ 1>(c); WAIT_V(9); } else { WAIT_V(0); }
  WAIT_L0();
  BAR();
  k1_mma(acc, fb, fa);
  BAR();
  k1_load<BUF, 1>(lds, aB, aA, fb, fa);
  if (MODE == 0) { if (GRP == 0) k1_issue_B<BUF>(c); else k1_issue_A<BUF>(c); WAIT_V(9); }
  else if (MODE == 1) { WAIT_V(4); }
  WAIT_L0();
  BAR();
  k1_mma(acc, fb, fa);
  if (!(MODE == 2 && GRP == 1)) BAR();
}
template <int GRP>
DI void k1_main(LAS char* lds, K1Ctx& c, const unsigned (&aB)[10], const unsigned (&aA)[2], f32x4 (&acc)[10][4]) {
  if (GRP == 1) BAR();
#pragma unroll 1
  for (int t = 0; t < 14; t += 2) {
    k1_tile<GRP, 0, 0>(lds, c, aB, aA, acc);
    k1_tile<GRP, 1, 0>(lds, c, aB, aA, acc);
  }
  k1_tile<GRP, 0, 1>(lds, c, aB, aA, acc);
  k1_tile<GRP, 1, 2>(lds, c, aB, aA, acc);
  MFMA_DRAIN();
}

DI void epi_h(const f32x4 (&ga)[4], const f32x4 (&ua)[4], unsigned short* hp  ) {
#pragma unroll
  for (int m = 0; m < 4; ++m) {
    u32x2 w;
    w[0] = pk2(gelu_mul(ga[m][0], ua[m][0]), gelu_mul(ga[m][1], ua[m][1]));
    w[1] = pk2(gelu_mul(ga[m][2], ua[m][2]), gelu_mul(ga[m][3], ua[m][3]));
    *(u32x2*)(hp + (size_t)m * 16 * F_) = w;
  }
}
DI void epi_q(const f32x4 (&a0)[4], const f32x4 (&a1)[4], unsigned short* qp  ) {
#pragma unroll
  for (int m = 0; m < 4; ++m) {
    u32x2 w0 = {pk2(a0[m][0], a0[m][1]), pk2(a0[m][2], a0[m][3])};
    u32x2 w1 = {pk2(a1[m][0], a1[m][1]), pk2(a1[m][2], a1[m][3])};
    *(u32x2*)(qp + (size_t)m * 16 * D_) = w0;
    *(u32x2*)(qp + (size_t)m * 16 * D_ + 16) = w1;
  }
}
DI void epi_kv(const bf16x8 (&kp)[4], const bf16x8 (&vp)[4],
               LAS char* wphi  , LAS char* red  , int lane) {
  const int g = lane >> 4, c = lane & 15, q = (lane >> 2) & 3, p = lane & 3;
  bf16x8 wf[2];
#pragma unroll
  for (int fb = 0; fb < 2; ++fb) {
    LAS char* a = wphi + (4 * g + q) * 64 + fb * 32 + p * 8;
    s16x4 lo = __builtin_amdgcn_ds_read_tr16_b64_v4i16((LAS s16x4*)a);
    s16x4 hi = __builtin_amdgcn_ds_read_tr16_b64_v4i16((LAS s16x4*)(a + 1024));
    wf[fb] = __builtin_shufflevector(lo, hi, 0, 1, 2, 3, 4, 5, 6, 7);
  }
  const bool hit = (c >> 2) == g;
  const unsigned one_lo = hit ? ((c & 1) ? 0x3F800000u : 0x00003F80u) : 0u;
  const unsigned w0 = (c & 2) ? 0u : one_lo, w1 = (c & 2) ? one_lo : 0u;
  const u32x4 i0 = {w0, w1, 0u, 0u}, i1 = {0u, 0u, w0, w1};
  const bf16x8 I0 = __builtin_bit_cast(bf16x8, i0), I1 = __builtin_bit_cast(bf16x8, i1);
  const u32x4 onesu = {0x3F803F80u, 0x3F803F80u, 0x3F803F80u, 0x3F803F80u};
  const bf16x8 ONES = __builtin_bit_cast(bf16x8, onesu);
  const f32x4 Z = {0.f, 0.f, 0.f, 0.f};
  f32x4 kv[4][3];
#pragma unroll
  for (int a = 0; a < 4; ++a)
#pragma unroll
    for (int b = 0; b < 3; ++b) kv[a][b] = Z;
#pragma unroll
  for (int mp = 0; mp < 2; ++mp) {
    f32x4 zp[2][4], zv[2][2];
#pragma unroll
    for (int h = 0; h < 2; ++h) {
      const int m = 2 * mp + h;
      const bf16x8 kf = kp[m];
      const f32x4 p0 = MFMA(wf[0], kf, Z), p1 = MFMA(wf[1], kf, Z);
      bf16x8 fp, fn;
      feature_map(p0, p1, fp, fn);
      zp[h][0] = MFMA(fp, I0, Z); zp[h][1] = MFMA(fp, I1, Z);
      zp[h][2] = MFMA(fn, I0, Z); zp[h][3] = MFMA(fn, I1, Z);
      const bf16x8 vf = vp[m];
      zv[h][0] = MFMA(vf, I0, Z); zv[h][1] = MFMA(vf, I1, Z);
    }
    const bf16x8 vb0 = pack8(zv[0][0], zv[1][0]), vb1 = pack8(zv[0][1], zv[1][1]);
#pragma unroll
    for (int a = 0; a < 4; ++a) {
      const bf16x8 pa = pack8(zp[0][a], zp[1][a]);
      kv[a][0] = MFMA(pa, vb0, kv[a][0]);
      kv[a][1] = MFMA(pa, vb1, kv[a][1]);
      kv[a][2] = MFMA(pa, ONES, kv[a][2]);
    }
  }
#pragma unroll
  for (int a = 0; a < 4; ++a) {
    *(LAS f32x4*)(red + (a * 2 + 0) * 1024 + lane * 16) = kv[a][0];
    *(LAS f32x4*)(red + (a * 2 + 1) * 1024 + lane * 16) = kv[a][1];
    if (c == 0) *(LAS f32x4*)(red + 8192 + a * 64 + g * 16) = kv[a][2];
  }
}

__global__ __launch_bounds__(512, 2) void k1_kernel(const unsigned short* __restrict__ x, const unsigned short* __restrict__ wqkv,
                                                    const unsigned short* __restrict__ wphi, const unsigned short* __restrict__ wg,
                                                    const unsigned short* __restrict__ wu, const unsigned short* __restrict__ gatew,
                                                    unsigned short* __restrict__ hbuf, unsigned short* __restrict__ qbuf,
                                                    float* __restrict__ kvp, float* __restrict__ gate) {
  extern __shared__ __attribute__((aligned(16))) char smem[];
  LAS char* lds = (LAS char*)smem;
  const unsigned ldsb = (unsigned)(size_t)lds;
  const int tid = threadIdx.x, lane = tid & 63;
  const int wid = __builtin_amdgcn_readfirstlane(tid >> 6);
  const int grp = wid >> 2, j = wid & 3, wm = 2 * grp + (j >> 1), wn = j & 1;
  const int bid = blockIdx.x, xcd = bid & 7, idx = bid >> 3;
  const int rt = idx & 7, cg = xcd * 4 + (idx >> 3);
  const int row0 = rt * 256;
  const int g = lane >> 4, c = lane & 15;

  u32x4 gx0, gx1, gw0, gw1;
  {
    const char* xr = (const char*)x + (size_t)(bid * 8 + wid) * 2048;
    const unsigned vo = lane * 16;
    asm volatile("global_load_dwordx4 %0, %4, %5\n\tglobal_load_dwordx4 %1, %4, %5 offset:1024\n\t"
                 "global_load_dwordx4 %2, %4, %6\n\tglobal_load_dwordx4 %3, %4, %6 offset:1024"
                 : "=&v"(gx0), "=&v"(gx1), "=&v"(gw0), "=&v"(gw1) : "v"(vo), "s"(xr), "s"(gatew) : "memory");
  }

  const int ttype = cg < 22 ? 0 : (cg == 22 ? 1 : (cg == 23 ? 2 : 3));
  const unsigned SW = F_ * 2, SQ = 3 * D_ * 2;
  const char* pb[4]; unsigned ps[4]; const char* n0; const char* n1; unsigned ns;
  {
    const int odd = cg & 1, jj = cg - 24;
    const int wc = cg * 160 + (odd ? 32 : 0), nc = cg * 160 + (odd ? 0 : 128);
    const char* G = (const char*)wg; const char* U = (const char*)wu; const char* Q = (const char*)wqkv;
    const int o0 = ttype == 0 ? wc : (ttype == 1 ? 3520 : (ttype == 2 ? 512 : 1024 + 128 * jj));
    const int o1 = ttype == 0 ? wc + 64 : (ttype == 1 ? 3520 : (ttype == 2 ? 576 : 1088 + 128 * jj));
    const int o2 = ttype == 0 ? wc : (ttype == 1 ? 832 : (ttype == 2 ? 640 : 2048 + 128 * jj));
    const int o3 = ttype == 0 ? wc + 64 : (ttype == 1 ? 896 : (ttype == 2 ? 704 : 2112 + 128 * jj));
    const int o4 = ttype == 0 ? nc : (ttype == 1 ? 960 : (ttype == 2 ? 768 : 64 * jj));
    const int o5 = ttype == 0 ? nc : o4 + 32;
    pb[0] = (ttype <= 1 ? G : Q) + 2 * o0;
    pb[1] = (ttype == 0 ? G : (ttype == 1 ? U : Q)) + 2 * o1;
    pb[2] = (ttype == 0 ? U : Q) + 2 * o2;
    pb[3] = (ttype == 0 ? U : Q) + 2 * o3;
    n0 = (ttype == 0 ? G : Q) + 2 * o4;
    n1 = (ttype == 0 ? U : Q) + 2 * o5;
    ps[0] = ps[1] = ttype <= 1 ? SW : SQ;
    ps[2] = ps[3] = ns = ttype == 0 ? SW : SQ;
  }

  K1Ctx cx;
  {
    const int ar = (grp == 0 ? 128 : 0) + 32 * j;
    cx.sA = uni_ptr((const char*)x + (unsigned)(row0 + ar) * 2048u);
    cx.ldsA = ldsb + K1_A0 + ar * 128;
    const int r = lane >> 3, ch = lane & 7;
    cx.voffA0 = r * 2048 + ((ch ^ (r >> 1)) * 16);
    cx.voffA1 = cx.voffA0 ^ 64;
    const int kr = grp * 32 + 8 * j;
    const int swz = ((r >> 1) & 1) | ((j & 1) << 1);
    const int l32 = (ch >> 1) ^ swz;
    const unsigned coff = l32 * 32 + (ch & 1) * 16;
#pragma unroll
    for (int i = 0; i < 4; ++i) { cx.sB[i] = uni_ptr(pb[i] + (unsigned)kr * ps[i]); cx.stepB[i] = 64 * ps[i]; cx.voffB[i] = r * ps[i] + coff; }
    cx.vB4 = ((l32 & 2) ? n1 : n0) + (size_t)(kr + r) * ns + (coff & 63);
    cx.stepB4 = 64 * ns;
    cx.ldsB = ldsb + K1_B0 + kr * 128;
  }
  if (ttype == 3) {
    glds_s(lane * 16, (const char*)wphi + (size_t)(cg - 24) * 8192 + wid * 1024, ldsb + K1_WPHI + wid * 1024);
  }
  if (grp == 0) { k1_issue_B<0>(cx); k1_issue_A<0>(cx); k1_issue_B<1>(cx); }
  else          { k1_issue_A<0>(cx); k1_issue_B<0>(cx); k1_issue_A<1>(cx); }

  unsigned aB[10], aA[2];
  {
    const int q = (lane >> 2) & 3, p = lane & 3;
    const int swz = ((q >> 1) & 1) | ((g & 1) << 1);
    const unsigned lb = K1_B0 + (8 * g + q) * 128 + p * 8;
    const int var = ttype == 0 ? (cg & 1) : ttype + 1;
    const unsigned long long t0 = var == 0 ? k1_tab(0, 0) : var == 1 ? k1_tab(1, 0) : var == 2 ? k1_tab(2, 0) : var == 3 ? k1_tab(3, 0) : k1_tab(4, 0);
    const unsigned long long t1 = var == 0 ? k1_tab(0, 1) : var == 1 ? k1_tab(1, 1) : var == 2 ? k1_tab(2, 1) : var == 3 ? k1_tab(3, 1) : k1_tab(4, 1);
    const unsigned long long tb = wn ? t1 : t0;
#pragma unroll
    for (int i = 0; i < 10; ++i) {
      const unsigned code = (unsigned)(tb >> (5 * i)) & 31u;
      aB[i] = lb + (code >> 2) * 8192 + (((code & 3) ^ swz) * 32);
    }
    const int sa = (c >> 1) & 7;
    aA[0] = (64 * wm + c) * 128 + ((g ^ sa) * 16);
    aA[1] = aA[0] ^ 64;
  }

  {
    asm volatile("s_waitcnt vmcnt(13)" : "+v"(gx0), "+v"(gx1), "+v"(gw0), "+v"(gw1)::"memory");
    float s = 0.f;
#pragma unroll
    for (int e = 0; e < 4; ++e) {
      s = __builtin_fmaf(__uint_as_float(gx0[e] << 16), __uint_as_float(gw0[e] << 16), s);
      s = __builtin_fmaf(__uint_as_float(gx0[e] & 0xffff0000u), __uint_as_float(gw0[e] & 0xffff0000u), s);
      s = __builtin_fmaf(__uint_as_float(gx1[e] << 16), __uint_as_float(gw1[e] << 16), s);
      s = __builtin_fmaf(__uint_as_float(gx1[e] & 0xffff0000u), __uint_as_float(gw1[e] & 0xffff0000u), s);
    }
#pragma unroll
    for (int o = 1; o < 64; o <<= 1) s += __shfl_xor(s, o);
    const float gv = 1.0f / (1.0f + __expf(-s));
    if (lane == 0) {
      float* gp = gate + bid * 8 + wid;
      asm volatile("global_store_dword %0, %1, %2" ::"v"(0u), "v"(gv), "s"(gp) : "memory");
    }
  }

  f32x4 acc[10][4];
#pragma unroll
  for (int i = 0; i < 10; ++i)
#pragma unroll
    for (int m = 0; m < 4; ++m) acc[i][m] = (f32x4){0.f, 0.f, 0.f, 0.f};

  WAIT_V(9);
  BAR();
  if (grp == 0) k1_main<0>(lds, cx, aB, aA, acc); else k1_main<1>(lds, cx, aB, aA, acc);

  int tz = threadIdx.x;
  asm volatile("" : "+v"(tz));
  const int lane2 = tz & 63, g2 = lane2 >> 4;
  const int rowl = row0 + 64 * wm + (lane2 & 15);
  const int jj = cg - 24;
  {
    unsigned short* hp = hbuf + (size_t)rowl * F_ + 4 * g2 + (ttype == 0 ? cg * 160 + 80 * wn : 3520 + 32 * wn);
    unsigned short* qp = qbuf + (size_t)rowl * D_ + 4 * g2 + 32 * (ttype == 1 ? 24 + 3 * wn : (ttype == 2 ? 16 + 5 * wn : 2 * jj + wn - 4));
#pragma unroll
    for (int s = 4; s >= 0; --s) {
      const bool isH = ttype == 0 || (ttype == 1 && s < 2);
      const bool isQ = ttype == 2 || (ttype == 1 && s >= 2) || (ttype == 3 && s == 4);
      if (isH) epi_h(acc[2 * s], acc[2 * s + 1], hp + 16 * s);
      else if (isQ) epi_q(acc[2 * s], acc[2 * s + 1], qp + 32 * s);
    }
  }
  if (ttype == 3) {
    LAS char* red = lds + (wm * 4 + 2 * wn) * 8448;
    bf16x8 pk[4][4];
#pragma unroll
    for (int s = 0; s < 4; ++s)
#pragma unroll
      for (int m = 0; m < 4; ++m) { pk[s][m] = pack8(acc[2 * s][m], acc[2 * s + 1][m]); asm volatile("" : "+v"(pk[s][m])); }
    epi_kv(pk[0], pk[1], lds + K1_WPHI + (2 * wn) * 2048, red, lane2);
    epi_kv(pk[2], pk[3], lds + K1_WPHI + (2 * wn + 1) * 2048, red + 8448, lane2);
    __syncthreads();
    for (int e = tz; e < 4 * 528; e += 512) {
      LAS char* p = lds + e * 16;
      f32x4 s = *(LAS f32x4*)p;
      s += *(LAS f32x4*)(p + 4 * 8448);
      s += *(LAS f32x4*)(p + 8 * 8448);
      s += *(LAS f32x4*)(p + 12 * 8448);
      *(f32x4*)(kvp + ((size_t)rt * 32 + 4 * jj) * KVP + e * 4) = s;
    }
  }
}

__global__ __launch_bounds__(512) void ko_kernel(const unsigned short* __restrict__ qbuf, const unsigned short* __restrict__ wphi,
                                                 const float* __restrict__ kvp, const float* __restrict__ gate,
                                                 unsigned short* __restrict__ obuf) {
  __shared__ __attribute__((aligned(16))) char sm[528 * 8 + 2048];
  LAS char* lds = (LAS char*)sm;
  const int tid = threadIdx.x, lane = tid & 63, wid = tid >> 6;
  const int head = blockIdx.x & 31, rb = blockIdx.x >> 5;
  const int g = lane >> 4, c = lane & 15, q = (lane >> 2) & 3, p = lane & 3;
  bf16x8 qf[2];
  float gt[2];
#pragma unroll
  for (int m = 0; m < 2; ++m) {
    const int row = rb * 256 + wid * 32 + m * 16 + c;
    qf[m] = *(const bf16x8*)(qbuf + (size_t)row * D_ + head * 32 + g * 8);
    gt[m] = gate[row];
  }
  for (int e = tid; e < 528; e += 512) {
    f32x4 s = {0.f, 0.f, 0.f, 0.f};
#pragma unroll
    for (int r = 0; r < 8; ++r) s += *(const f32x4*)(kvp + ((size_t)r * 32 + head) * KVP + e * 4);
    u32x2 w = {pk2(s[0], s[1]), pk2(s[2], s[3])};
    *(LAS u32x2*)(lds + e * 8) = w;
  }
  if (tid < 128) *(LAS u32x4*)(lds + 528 * 8 + tid * 16) = *(const u32x4*)((const char*)wphi + (size_t)head * 2048 + tid * 16);
  __syncthreads();
  bf16x8 wf[2];
#pragma unroll
  for (int fb = 0; fb < 2; ++fb) {
    LAS char* a = lds + 528 * 8 + (8 * g + q) * 64 + fb * 32 + p * 8;
    s16x4 lo = __builtin_amdgcn_ds_read_tr16_b64_v4i16((LAS s16x4*)a);
    s16x4 hi = __builtin_amdgcn_ds_read_tr16_b64_v4i16((LAS s16x4*)(a + 256));
    wf[fb] = __builtin_shufflevector(lo, hi, 0, 1, 2, 3, 4, 5, 6, 7);
  }
  bf16x8 kf[2][2], zf[2];
#pragma unroll
  for (int h = 0; h < 2; ++h) {
#pragma unroll
    for (int db = 0; db < 2; ++db) {
      const u32x2 lo = *(LAS u32x2*)(lds + (((2 * h) * 2 + db) * 64 + lane) * 8);
      const u32x2 hi = *(LAS u32x2*)(lds + (((2 * h + 1) * 2 + db) * 64 + lane) * 8);
      const u32x4 w = {lo[0], lo[1], hi[0], hi[1]};
      kf[h][db] = __builtin_bit_cast(bf16x8, w);
    }
    const u32x2 lo = *(LAS u32x2*)(lds + (512 + (2 * h) * 4 + g) * 8);
    const u32x2 hi = *(LAS u32x2*)(lds + (512 + (2 * h + 1) * 4 + g) * 8);
    const u32x4 w = {lo[0], lo[1], hi[0], hi[1]};
    zf[h] = __builtin_bit_cast(bf16x8, w);
  }
  const f32x4 Z = {0.f, 0.f, 0.f, 0.f};
#pragma unroll
  for (int m = 0; m < 2; ++m) {
    const f32x4 p0 = MFMA(wf[0], qf[m], Z), p1 = MFMA(wf[1], qf[m], Z);
    bf16x8 fp, fn;
    feature_map(p0, p1, fp, fn);
    f32x4 n0 = MFMA(kf[0][0], fp, Z); n0 = MFMA(kf[1][0], fn, n0);
    f32x4 n1 = MFMA(kf[0][1], fp, Z); n1 = MFMA(kf[1][1], fn, n1);
    f32x4 dn = MFMA(zf[0], fp, Z); dn = MFMA(zf[1], fn, dn);
    const float sc = gt[m] / (dn[0] + 1e-6f);
    n0 *= sc; n1 *= sc;
    const int row = rb * 256 + wid * 32 + m * 16 + c;
    unsigned short* op = obuf + (size_t)row * D_ + head * 32 + 4 * g;
    u32x2 w0 = {pk2(n0[0], n0[1]), pk2(n0[2], n0[3])}, w1 = {pk2(n1[0], n1[1]), pk2(n1[2], n1[3])};
    *(u32x2*)op = w0;
    *(u32x2*)(op + 16) = w1;
  }
}

constexpr int K2_ST = 24576, K2_NST = 6, K2_LDS = K2_ST * K2_NST;
__global__ __launch_bounds__(512, 2) void k2_kernel(const unsigned short* __restrict__ hbuf, const unsigned short* __restrict__ obuf,
                                                    const unsigned short* __restrict__ wd, const unsigned short* __restrict__ wo,
                                                    const float* __restrict__ gate, unsigned short* __restrict__ out) {
  extern __shared__ __attribute__((aligned(16))) char smem[];
  LAS char* lds = (LAS char*)smem;
  const unsigned ldsb = (unsigned)(size_t)lds;
  const int tid = threadIdx.x, lane = tid & 63;
  const int wid = __builtin_amdgcn_readfirstlane(tid >> 6);
  const int bid = blockIdx.x, xcd = bid & 7, idx = bid >> 3;
  const int tm = (xcd >> 1) * 4 + (idx & 3), tn = (xcd & 1) * 8 + (idx >> 2);
  const int wm = wid >> 1, wn = wid & 1;
  const int g = lane >> 4, c = lane & 15;
  const int r = lane >> 3, ch = lane & 7;
  const unsigned swA = ((ch ^ (r >> 1)) * 16);
  const unsigned voffAh0 = r * (F_ * 2) + swA, voffAh1 = voffAh0 ^ 64;
  const unsigned voffAo0 = r * (D_ * 2) + swA, voffAo1 = voffAo0 ^ 64;
  const int swzb = ((r >> 1) & 1) | ((wid & 1) << 1);
  const unsigned voffB = r * 2048 + (((ch >> 1) ^ swzb) * 32) + (ch & 1) * 16;
  const char* aH = (const char*)hbuf + (size_t)(tm * 128 + 16 * wid) * (F_ * 2);
  const char* aO = (const char*)obuf + (size_t)(tm * 128 + 16 * wid) * (D_ * 2);
  const char* bD = (const char*)wd + (size_t)(8 * wid) * 2048 + tn * 128;
  const char* bO = (const char*)wo + (size_t)(8 * wid) * 2048 + tn * 128;
  auto issue = [&](int t, int st) {
    const unsigned la = ldsb + st * K2_ST + wid * 2048, lb = ldsb + st * K2_ST + 16384 + wid * 1024;
    if (t < 56) {
      glds_s(voffAh0, aH + t * 128, la); glds_s(voffAh1, aH + t * 128 + 8 * (F_ * 2), la + 1024);
      glds_s(voffB, bD + (size_t)t * 64 * 2048, lb);
    } else {
      const int u = t < 72 ? t - 56 : 0;
      glds_s(voffAo0, aO + u * 128, la); glds_s(voffAo1, aO + u * 128 + 8 * (D_ * 2), la + 1024);
      glds_s(voffB, bO + (size_t)u * 64 * 2048, lb);
    }
  };
  const int q = (lane >> 2) & 3, p = lane & 3;
  const int swz = ((q >> 1) & 1) | ((g & 1) << 1);
  unsigned aB[2], aA[2];
  aB[0] = 16384 + (8 * g + q) * 128 + p * 8 + (((2 * wn) ^ swz) * 32);
  aB[1] = 16384 + (8 * g + q) * 128 + p * 8 + (((2 * wn + 1) ^ swz) * 32);
  aA[0] = (32 * wm + c) * 128 + ((g ^ ((c >> 1) & 7)) * 16);
  aA[1] = aA[0] ^ 64;
  float gt[2];
  gt[0] = 1.0f - gate[tm * 128 + 32 * wm + c];
  gt[1] = 1.0f - gate[tm * 128 + 32 * wm + 16 + c];
  asm volatile("" : "+v"(gt[0]), "+v"(gt[1]));
  f32x4 acc[2][2];
#pragma unroll
  for (int i = 0; i < 2; ++i)
#pragma unroll
    for (int m = 0; m < 2; ++m) acc[i][m] = (f32x4){0.f, 0.f, 0.f, 0.f};
#pragma unroll
  for (int t = 0; t < 5; ++t) issue(t, t);
  int st = 0, sti = 5;
#pragma unroll 1
  for (int t = 0; t < 72; ++t) {
    WAIT_V(12);
    BAR();
    issue(t + 5, sti);
    LAS char* sb = lds + st * K2_ST;
    if (t == 56) {
#pragma unroll
      for (int i = 0; i < 2; ++i)
#pragma unroll
        for (int m = 0; m < 2; ++m) acc[i][m] *= gt[m];
    }
#pragma unroll
    for (int ks = 0; ks < 2; ++ks) {
      bf16x8 fa[2], fb[2];
#pragma unroll
      for (int m = 0; m < 2; ++m) fa[m] = *(LAS bf16x8*)(sb + aA[ks] + m * 2048);
#pragma unroll
      for (int i = 0; i < 2; ++i) fb[i] = tr_frag(sb + aB[i], ks * 4096);
#pragma unroll
      for (int i = 0; i < 2; ++i)
#pragma unroll
        for (int m = 0; m < 2; ++m) acc[i][m] = MFMA(fb[i], fa[m], acc[i][m]);
    }
    WAIT_L0();
    st = (st == K2_NST - 1) ? 0 : st + 1;
    sti = (sti == K2_NST - 1) ? 0 : sti + 1;
  }
  WAIT_V(0);
#pragma unroll
  for (int m = 0; m < 2; ++m) {
    unsigned short* op = out + (size_t)(tm * 128 + 32 * wm + 16 * m + c) * D_ + tn * 64 + 32 * wn + 4 * g;
#pragma unroll
    for (int i = 0; i < 2; ++i) {
      u32x2 w = {pk2(acc[i][m][0], acc[i][m][1]), pk2(acc[i][m][2], acc[i][m][3])};
      *(u32x2*)(op + 16 * i) = w;
    }
  }
}

extern "C" void kernel_launch(void* const* d_in, const int* in_sizes, int n_in, void* d_out, int out_size, void* d_ws,
                              size_t ws_size, hipStream_t stream) {
  const unsigned short* x = (const unsigned short*)d_in[0];
  const unsigned short* wqkv = (const unsigned short*)d_in[1];
  const unsigned short* wphi = (const unsigned short*)d_in[2];
  const unsigned short* wo = (const unsigned short*)d_in[3];
  const unsigned short* wg = (const unsigned short*)d_in[5];
  const unsigned short* wu = (const unsigned short*)d_in[6];
  const unsigned short* wd = (const unsigned short*)d_in[7];
  const unsigned short* gatew = (const unsigned short*)d_in[8];
  char* ws = (char*)d_ws;
  unsigned short* hbuf = (unsigned short*)ws;
  unsigned short* qbuf = (unsigned short*)(ws + (16u << 20));
  unsigned short* obuf = (unsigned short*)(ws + (20u << 20));
  float* kvp = (float*)(ws + (24u << 20));
  float* gate = (float*)(ws + (27u << 20));
  (void)hipFuncSetAttribute((const void*)k1_kernel, hipFuncAttributeMaxDynamicSharedMemorySize, K1_LDS);
  (void)hipFuncSetAttribute((const void*)k2_kernel, hipFuncAttributeMaxDynamicSharedMemorySize, K2_LDS);
  k1_kernel<<<256, 512, K1_LDS, stream>>>(x, wqkv, wphi, wg, wu, gatew, hbuf, qbuf, kvp, gate);
  ko_kernel<<<256, 512, 0, stream>>>(qbuf, wphi, kvp, gate, obuf);
  k2_kernel<<<256, 512, K2_LDS, stream>>>(hbuf, obuf, wd, wo, gate, (unsigned short*)d_out);
}
```

```cpp
#include <hip/hip_runtime.h>
#include <hip/hip_bf16.h>
#include <stdint.h>

typedef short bf16x8 __attribute__((ext_vector_type(8)));
typedef short s16x4 __attribute__((ext_vector_type(4)));
typedef float f32x4 __attribute__((ext_vector_type(4)));
typedef float f32x2 __attribute__((ext_vector_type(2)));
typedef unsigned u32x4 __attribute__((ext_vector_type(4)));
typedef unsigned u32x2 __attribute__((ext_vector_type(2)));
typedef __bf16 bf16x2_t __attribute__((ext_vector_type(2)));
#define LAS __attribute__((address_space(3)))
#define GAS __attribute__((address_space(1)))
#define DI __device__ __forceinline__

constexpr int S_ = 2048, D_ = 1024, F_ = 3584;
constexpr int PANEL = S_ * 32;
constexpr int KVP = 2112;

DI void glds_s(unsigned voff, const void* sbase, unsigned ldsaddr) {
  asm volatile("s_mov_b32 m0, %2\n\ts_nop 0\n\tglobal_load_lds_dwordx4 %0, %1" ::"v"(voff), "s"(sbase), "s"(ldsaddr) : "memory");
}
DI void glds_v(const void* vaddr, unsigned ldsaddr) {
  asm volatile("s_mov_b32 m0, %1\n\ts_nop 0\n\tglobal_load_lds_dwordx4 %0, off" ::"v"(vaddr), "s"(ldsaddr) : "memory");
}
DI const char* uni_ptr(const char* p) {
  const unsigned long long v = (unsigned long long)p;
  const unsigned lo = __builtin_amdgcn_readfirstlane((unsigned)v), hi = __builtin_amdgcn_readfirstlane((unsigned)(v >> 32));
  return (const char*)(((unsigned long long)hi << 32) | lo);
}
#define WAIT_V(n) asm volatile("s_waitcnt vmcnt(" #n ")" ::: "memory")
#define WAIT_L0() __builtin_amdgcn_s_waitcnt(0xC07F)
#define SCHED() __builtin_amdgcn_sched_barrier(0)
#define BAR() do { SCHED(); asm volatile("" ::: "memory"); __builtin_amdgcn_s_barrier(); asm volatile("" ::: "memory"); SCHED(); } while (0)
#define MFMA_ACC(c, a, b) asm volatile("v_mfma_f32_16x16x32_bf16 %0, %1, %2, %0" : "+v"(c) : "v"(a), "v"(b))
#define MFMA_DRAIN() do { SCHED(); asm volatile("s_nop 7\n\ts_nop 7" ::: "memory"); SCHED(); } while (0)
#define MFMA(a, b, c) __builtin_amdgcn_mfma_f32_16x16x32_bf16((a), (b), (c), 0, 0, 0)

DI unsigned pk2(float lo, float hi) {
  f32x2 v = {lo, hi};
  bf16x2_t r = __builtin_convertvector(v, bf16x2_t);
  return __builtin_bit_cast(unsigned, r);
}
DI bf16x8 pack8(const f32x4& a, const f32x4& b) {
  u32x4 p = {pk2(a[0], a[1]), pk2(a[2], a[3]), pk2(b[0], b[1]), pk2(b[2], b[3])};
  return __builtin_bit_cast(bf16x8, p);
}
DI bf16x8 tr_frag(LAS char* p, int off) {
  s16x4 a = __builtin_amdgcn_ds_read_tr16_b64_v4i16((LAS s16x4*)(p + off));
  s16x4 b = __builtin_amdgcn_ds_read_tr16_b64_v4i16((LAS s16x4*)(p + off + 512));
  return __builtin_shufflevector(a, b, 0, 1, 2, 3, 4, 5, 6, 7);
}
DI float bf2f(unsigned short u) { return __uint_as_float(((unsigned)u) << 16); }
DI float gelu_mul(float x, float u) {
  const float c0 = -2.3022081984f;
  const float c1 = -2.3022081984f * 0.044715f;
  float t = x * x;
  float p = __builtin_fmaf(t, c1, c0);
  float e = __builtin_amdgcn_exp2f(x * p);
  float r = __builtin_amdgcn_rcpf(1.0f + e);
  return x * r * u;
}
DI void swap16(float& a, float& b) {
  auto r = __builtin_amdgcn_permlane16_swap(__float_as_uint(a), __float_as_uint(b), false, false);
  a = __uint_as_float(r[0]); b = __uint_as_float(r[1]);
}
DI void swap32(float& a, float& b) {
  auto r = __builtin_amdgcn_permlane32_swap(__float_as_uint(a), __float_as_uint(b), false, false);
  a = __uint_as_float(r[0]); b = __uint_as_float(r[1]);
}
template <bool MAX> DI float rop(float a, float b) { return MAX ? fmaxf(a, b) : a + b; }
template <bool MAX> DI void xrow4(float& a, float& b, float& c, float& d) {
  swap32(a, b); float r1 = rop<MAX>(a, b);
  swap32(c, d); float r2 = rop<MAX>(c, d);
  swap16(r1, r2); float r = rop<MAX>(r1, r2);
  float u = r, v = r; swap16(u, v);
  a = u; b = u; swap32(a, b);
  c = v; d = v; swap32(c, d);
}
DI u32x4 widen(u32x2 a, u32x2 b) {
  auto r0 = __builtin_amdgcn_permlane16_swap(a[0], b[0], false, false);
  auto r1 = __builtin_amdgcn_permlane16_swap(a[1], b[1], false, false);
  u32x4 w = {r0[0], r1[0], r0[1], r1[1]};
  return w;
}
DI u32x2 pk4(const f32x4& v) { u32x2 w = {pk2(v[0], v[1]), pk2(v[2], v[3])}; return w; }

DI void feature_map2(const f32x4 (&p)[2][2], bf16x8 (&fpos)[2], bf16x8 (&fneg)[2]) {
  const float L2E = 1.4426950408889634f;
  float mx[2], nm[2];
#pragma unroll
  for (int t = 0; t < 2; ++t) {
    const f32x4 &p0 = p[t][0], &p1 = p[t][1];
    mx[t] = fmaxf(fmaxf(fmaxf(p0[0], p0[1]), fmaxf(p0[2], p0[3])), fmaxf(fmaxf(p1[0], p1[1]), fmaxf(p1[2], p1[3])));
    nm[t] = -fminf(fminf(fminf(p0[0], p0[1]), fminf(p0[2], p0[3])), fminf(fminf(p1[0], p1[1]), fminf(p1[2], p1[3])));
  }
  xrow4<true>(mx[0], nm[0], mx[1], nm[1]);
  f32x4 e[2][2], n[2][2];
  float sp[2], sn[2];
#pragma unroll
  for (int t = 0; t < 2; ++t) {
    const float a = -mx[t] * L2E, b = -nm[t] * L2E;
#pragma unroll
    for (int h = 0; h < 2; ++h)
#pragma unroll
      for (int r = 0; r < 4; ++r) {
        e[t][h][r] = __builtin_amdgcn_exp2f(__builtin_fmaf(p[t][h][r], L2E, a));
        n[t][h][r] = __builtin_amdgcn_exp2f(__builtin_fmaf(p[t][h][r], -L2E, b));
      }
    sp[t] = ((e[t][0][0] + e[t][0][1]) + (e[t][0][2] + e[t][0][3])) + ((e[t][1][0] + e[t][1][1]) + (e[t][1][2] + e[t][1][3]));
    sn[t] = ((n[t][0][0] + n[t][0][1]) + (n[t][0][2] + n[t][0][3])) + ((n[t][1][0] + n[t][1][1]) + (n[t][1][2] + n[t][1][3]));
  }
  xrow4<false>(sp[0], sn[0], sp[1], sn[1]);
#pragma unroll
  for (int t = 0; t < 2; ++t) {
    const float rp = __builtin_amdgcn_rcpf(sp[t]), rn = __builtin_amdgcn_rcpf(sn[t]);
    fpos[t] = pack8(e[t][0] * rp, e[t][1] * rp);
    fneg[t] = pack8(n[t][0] * rn, n[t][1] * rn);
  }
}

constexpr int K1_A0 = 0, K1_ABUF = 32768, K1_B0 = 65536, K1_BBUF = 40960, K1_WPHI = 147456, K1_LDS = 147456 + 8192;

constexpr unsigned long long k1_tab(int var, int wn) {
  unsigned long long t = 0;
  for (int i = 0; i < 10; ++i) {
    const int s = i >> 1, half = i & 1;
    int panel = 0, c32 = 0;
    if (var <= 1) {
      const int b = 5 * wn + s;
      if (var == 1) { if (b < 2) { panel = 4; c32 = b + 2 * half; } else { panel = ((b - 2) >> 2) + 2 * half; c32 = (b - 2) & 3; } }
      else          { if (b >= 8) { panel = 4; c32 = b - 8 + 2 * half; } else { panel = (b >> 2) + 2 * half; c32 = b & 3; } }
    } else if (var == 2) {
      if (s < 2) { panel = half; c32 = 2 * wn + s; } else { const int u = 3 * wn + s - 2; panel = 2 + (u >> 1); c32 = 2 * (u & 1) + half; }
    } else if (var == 3) {
      const int u = 5 * wn + s; panel = u >> 1; c32 = 2 * (u & 1) + half;
    } else {
      if (s == 4) { panel = 4; c32 = 2 * wn + half; } else { panel = (s & 1) * 2 + wn; c32 = (s >> 1) * 2 + half; }
    }
    t |= (unsigned long long)(panel * 4 + c32) << (5 * i);
  }
  return t;
}

struct K1Ctx {
  const char* sA;
  const char* sB[4];
  unsigned stepB[4];
  const char* vB4;
  unsigned stepB4;
  unsigned voffA0, voffA1, voffB[4];
  unsigned ldsA, ldsB;
};

template <int BUF> DI void k1_issue_A(K1Ctx& c) {
#pragma unroll
  for (int i = 0; i < 4; ++i) glds_s((i & 1) ? c.voffA1 : c.voffA0, c.sA + i * 16384, c.ldsA + BUF * K1_ABUF + i * 1024);
  c.sA += 128;
}
template <int BUF> DI void k1_issue_B(K1Ctx& c) {
#pragma unroll
  for (int i = 0; i < 4; ++i) { glds_s(c.voffB[i], c.sB[i], c.ldsB + BUF * K1_BBUF + i * 8192); c.sB[i] += c.stepB[i]; }
  glds_v(c.vB4, c.ldsB + BUF * K1_BBUF + 4 * 8192); c.vB4 += c.stepB4;
}

template <int BUF, int KS> DI void k1_load(LAS char* lds, const unsigned (&aB)[10], const unsigned (&aA)[2], bf16x8 (&fb)[10], bf16x8 (&fa)[4]) {
#pragma unroll
  for (int m = 0; m < 4; ++m) fa[m] = *(LAS bf16x8*)(lds + aA[KS] + (K1_A0 + BUF * K1_ABUF + m * 2048));
#pragma unroll
  for (int i = 0; i < 10; ++i) fb[i] = tr_frag(lds + aB[i], BUF * K1_BBUF + KS * 4096);
}
DI void k1_mma(f32x4 (&acc)[10][4], const bf16x8 (&fb)[10], const bf16x8 (&fa)[4]) {
  __builtin_amdgcn_s_setprio(1);
#pragma unroll
  for (int i = 0; i < 10; ++i)
#pragma unroll
    for (int m = 0; m < 4; ++m) MFMA_ACC(acc[i][m], fb[i], fa[m]);
  __builtin_amdgcn_s_setprio(0);
}
template <int GRP, int BUF, int MODE>
DI void k1_tile(LAS char* lds, K1Ctx& c, const unsigned (&aB)[10], const unsigned (&aA)[2], f32x4 (&acc)[10][4]) {
  bf16x8 fb[10], fa[4];
  k1_load<BUF, 0>(lds, aB, aA, fb, fa);
  if (MODE < 2) { if (GRP == 0) k1_issue_A<BUF ^ 1>(c); else k1_issue_B<BUF ^ 1>(c); WAIT_V(9); } else { WAIT_V(0); }
  WAIT_L0();
  BAR();
  k1_mma(acc, fb, fa);
  BAR();
  k1_load<BUF, 1>(lds, aB, aA, fb, fa);
  if (MODE == 0) { if (GRP == 0) k1_issue_B<BUF>(c); else k1_issue_A<BUF>(c); WAIT_V(9); }
  else if (MODE == 1) { WAIT_V(4); }
  WAIT_L0();
  BAR();
  k1_mma(acc, fb, fa);
  if (!(MODE == 2 && GRP == 1)) BAR();
}
template <int GRP>
DI void k1_main(LAS char* lds, K1Ctx& c, const unsigned (&aB)[10], const unsigned (&aA)[2], f32x4 (&acc)[10][4]) {
  if (GRP == 1) BAR();
#pragma unroll 1
  for (int t = 0; t < 14; t += 2) {
    k1_tile<GRP, 0, 0>(lds, c, aB, aA, acc);
    k1_tile<GRP, 1, 0>(lds, c, aB, aA, acc);
  }
  k1_tile<GRP, 0, 1>(lds, c, aB, aA, acc);
  k1_tile<GRP, 1, 2>(lds, c, aB, aA, acc);
  MFMA_DRAIN();
}

DI void epi_h(const f32x4 (&ga)[4], const f32x4 (&ua)[4], char* pp) {
#pragma unroll
  for (int mp = 0; mp < 2; ++mp) {
    u32x2 w[2];
#pragma unroll
    for (int h = 0; h < 2; ++h) {
      const int m = 2 * mp + h;
      w[h][0] = pk2(gelu_mul(ga[m][0], ua[m][0]), gelu_mul(ga[m][1], ua[m][1]));
      w[h][1] = pk2(gelu_mul(ga[m][2], ua[m][2]), gelu_mul(ga[m][3], ua[m][3]));
    }
    *(u32x4*)(pp + mp * 1024) = widen(w[0], w[1]);
  }
}
DI void epi_q(const f32x4 (&a0)[4], const f32x4 (&a1)[4], char* pp) {
#pragma unroll
  for (int mp = 0; mp < 2; ++mp) {
    *(u32x4*)(pp + mp * 1024) = widen(pk4(a0[2 * mp]), pk4(a0[2 * mp + 1]));
    *(u32x4*)(pp + PANEL + mp * 1024) = widen(pk4(a1[2 * mp]), pk4(a1[2 * mp + 1]));
  }
}
DI void epi_kv(const bf16x8 (&kp)[4], const bf16x8 (&vp)[4],
               LAS char* wphi  , LAS char* red  , int lane) {
  const int g = lane >> 4, c = lane & 15, q = (lane >> 2) & 3, p = lane & 3;
  bf16x8 wf[2];
#pragma unroll
  for (int fb = 0; fb < 2; ++fb) {
    LAS char* a = wphi + (4 * g + q) * 64 + fb * 32 + p * 8;
    s16x4 lo = __builtin_amdgcn_ds_read_tr16_b64_v4i16((LAS s16x4*)a);
    s16x4 hi = __builtin_amdgcn_ds_read_tr16_b64_v4i16((LAS s16x4*)(a + 1024));
    wf[fb] = __builtin_shufflevector(lo, hi, 0, 1, 2, 3, 4, 5, 6, 7);
  }
  const bool hit = (c >> 2) == g;
  const unsigned one_lo = hit ? ((c & 1) ? 0x3F800000u : 0x00003F80u) : 0u;
  const unsigned w0 = (c & 2) ? 0u : one_lo, w1 = (c & 2) ? one_lo : 0u;
  const u32x4 i0 = {w0, w1, 0u, 0u}, i1 = {0u, 0u, w0, w1};
  const bf16x8 I0 = __builtin_bit_cast(bf16x8, i0), I1 = __builtin_bit_cast(bf16x8, i1);
  const u32x4 onesu = {0x3F803F80u, 0x3F803F80u, 0x3F803F80u, 0x3F803F80u};
  const bf16x8 ONES = __builtin_bit_cast(bf16x8, onesu);
  const f32x4 Z = {0.f, 0.f, 0.f, 0.f};
  f32x4 kv[4][3];
#pragma unroll
  for (int a = 0; a < 4; ++a)
#pragma unroll
    for (int b = 0; b < 3; ++b) kv[a][b] = Z;
#pragma unroll
  for (int mp = 0; mp < 2; ++mp) {
    f32x4 zp[2][4], zv[2][2], pp[2][2];
#pragma unroll
    for (int h = 0; h < 2; ++h) { pp[h][0] = MFMA(wf[0], kp[2 * mp + h], Z); pp[h][1] = MFMA(wf[1], kp[2 * mp + h], Z); }
#pragma unroll
    for (int h = 0; h < 2; ++h) { zv[h][0] = MFMA(vp[2 * mp + h], I0, Z); zv[h][1] = MFMA(vp[2 * mp + h], I1, Z); }
    bf16x8 fp[2], fn[2];
    feature_map2(pp, fp, fn);
#pragma unroll
    for (int h = 0; h < 2; ++h) {
      zp[h][0] = MFMA(fp[h], I0, Z); zp[h][1] = MFMA(fp[h], I1, Z);
      zp[h][2] = MFMA(fn[h], I0, Z); zp[h][3] = MFMA(fn[h], I1, Z);
    }
    const bf16x8 vb0 = pack8(zv[0][0], zv[1][0]), vb1 = pack8(zv[0][1], zv[1][1]);
#pragma unroll
    for (int a = 0; a < 4; ++a) {
      const bf16x8 pa = pack8(zp[0][a], zp[1][a]);
      kv[a][0] = MFMA(pa, vb0, kv[a][0]);
      kv[a][1] = MFMA(pa, vb1, kv[a][1]);
      kv[a][2] = MFMA(pa, ONES, kv[a][2]);
    }
  }
#pragma unroll
  for (int a = 0; a < 4; ++a) {
    *(LAS f32x4*)(red + (a * 2 + 0) * 1024 + lane * 16) = kv[a][0];
    *(LAS f32x4*)(red + (a * 2 + 1) * 1024 + lane * 16) = kv[a][1];
    if (c == 0) *(LAS f32x4*)(red + 8192 + a * 64 + g * 16) = kv[a][2];
  }
}

__global__ __launch_bounds__(512, 2) void k1_kernel(const unsigned short* __restrict__ x, const unsigned short* __restrict__ wqkv,
                                                    const unsigned short* __restrict__ wphi, const unsigned short* __restrict__ wg,
                                                    const unsigned short* __restrict__ wu, const unsigned short* __restrict__ gatew,
                                                    char* __restrict__ act, char* __restrict__ qbuf,
                                                    float* __restrict__ kvp, float* __restrict__ gate, unsigned* __restrict__ flags) {
  extern __shared__ __attribute__((aligned(16))) char smem[];
  LAS char* lds = (LAS char*)smem;
  const unsigned ldsb = (unsigned)(size_t)lds;
  const int tid = threadIdx.x, lane = tid & 63;
  const int wid = __builtin_amdgcn_readfirstlane(tid >> 6);
  const int grp = wid >> 2, j = wid & 3, wm = 2 * grp + (j >> 1), wn = j & 1;
  const int bid = blockIdx.x, xcd = bid & 7, idx = bid >> 3;
  const int rt = idx & 7, cg = xcd * 4 + (idx >> 3);
  const int row0 = rt * 256;
  const int g = lane >> 4, c = lane & 15;

  u32x4 gx0, gx1, gw0, gw1;
  {
    const char* xr = (const char*)x + (size_t)(bid * 8 + wid) * 2048;
    const unsigned vo = lane * 16;
    asm volatile("global_load_dwordx4 %0, %4, %5\n\tglobal_load_dwordx4 %1, %4, %5 offset:1024\n\t"
                 "global_load_dwordx4 %2, %4, %6\n\tglobal_load_dwordx4 %3, %4, %6 offset:1024"
                 : "=&v"(gx0), "=&v"(gx1), "=&v"(gw0), "=&v"(gw1) : "v"(vo), "s"(xr), "s"(gatew) : "memory");
  }

  const int ttype = cg < 22 ? 0 : (cg == 22 ? 1 : (cg == 23 ? 2 : 3));
  const unsigned SW = F_ * 2, SQ = 3 * D_ * 2;
  const char* pb[4]; unsigned ps[4]; const char* n0; const char* n1; unsigned ns;
  {
    const int odd = cg & 1, jj = cg - 24;
    const int wc = cg * 160 + (odd ? 32 : 0), nc = cg * 160 + (odd ? 0 : 128);
    const char* G = (const char*)wg; const char* U = (const char*)wu; const char* Q = (const char*)wqkv;
    const int o0 = ttype == 0 ? wc : (ttype == 1 ? 3520 : (ttype == 2 ? 512 : 1024 + 128 * jj));
    const int o1 = ttype == 0 ? wc + 64 : (ttype == 1 ? 3520 : (ttype == 2 ? 576 : 1088 + 128 * jj));
    const int o2 = ttype == 0 ? wc : (ttype == 1 ? 832 : (ttype == 2 ? 640 : 2048 + 128 * jj));
    const int o3 = ttype == 0 ? wc + 64 : (ttype == 1 ? 896 : (ttype == 2 ? 704 : 2112 + 128 * jj));
    const int o4 = ttype == 0 ? nc : (ttype == 1 ? 960 : (ttype == 2 ? 768 : 64 * jj));
    const int o5 = ttype == 0 ? nc : o4 + 32;
    pb[0] = (ttype <= 1 ? G : Q) + 2 * o0;
    pb[1] = (ttype == 0 ? G : (ttype == 1 ? U : Q)) + 2 * o1;
    pb[2] = (ttype == 0 ? U : Q) + 2 * o2;
    pb[3] = (ttype == 0 ? U : Q) + 2 * o3;
    n0 = (ttype == 0 ? G : Q) + 2 * o4;
    n1 = (ttype == 0 ? U : Q) + 2 * o5;
    ps[0] = ps[1] = ttype <= 1 ? SW : SQ;
    ps[2] = ps[3] = ns = ttype == 0 ? SW : SQ;
  }

  K1Ctx cx;
  {
    const int ar = (grp == 0 ? 128 : 0) + 32 * j;
    cx.sA = uni_ptr((const char*)x + (unsigned)(row0 + ar) * 2048u);
    cx.ldsA = ldsb + K1_A0 + ar * 128;
    const int r = lane >> 3, ch = lane & 7;
    cx.voffA0 = r * 2048 + ((ch ^ (r >> 1)) * 16);
    cx.voffA1 = cx.voffA0 ^ 64;
    const int kr = grp * 32 + 8 * j;
    const int swz = ((r >> 1) & 1) | ((j & 1) << 1);
    const int l32 = (ch >> 1) ^ swz;
    const unsigned coff = l32 * 32 + (ch & 1) * 16;
#pragma unroll
    for (int i = 0; i < 4; ++i) { cx.sB[i] = uni_ptr(pb[i] + (unsigned)kr * ps[i]); cx.stepB[i] = 64 * ps[i]; cx.voffB[i] = r * ps[i] + coff; }
    cx.vB4 = ((l32 & 2) ? n1 : n0) + (size_t)(kr + r) * ns + (coff & 63);
    cx.stepB4 = 64 * ns;
    cx.ldsB = ldsb + K1_B0 + kr * 128;
    asm volatile("s_nop 4" : "+s"(cx.sA), "+s"(cx.sB[0]), "+s"(cx.sB[1]), "+s"(cx.sB[2]), "+s"(cx.sB[3]));
  }
  if (ttype == 3) {
    glds_s(lane * 16, (const char*)wphi + (size_t)(cg - 24) * 8192 + wid * 1024, ldsb + K1_WPHI + wid * 1024);
  }
  if (grp == 0) { k1_issue_B<0>(cx); k1_issue_A<0>(cx); k1_issue_B<1>(cx); }
  else          { k1_issue_A<0>(cx); k1_issue_B<0>(cx); k1_issue_A<1>(cx); }

  unsigned aB[10], aA[2];
  {
    const int q = (lane >> 2) & 3, p = lane & 3;
    const int swz = ((q >> 1) & 1) | ((g & 1) << 1);
    const unsigned lb = K1_B0 + (8 * g + q) * 128 + p * 8;
    const int var = ttype == 0 ? (cg & 1) : ttype + 1;
    const unsigned long long t0 = var == 0 ? k1_tab(0, 0) : var == 1 ? k1_tab(1, 0) : var == 2 ? k1_tab(2, 0) : var == 3 ? k1_tab(3, 0) : k1_tab(4, 0);
    const unsigned long long t1 = var == 0 ? k1_tab(0, 1) : var == 1 ? k1_tab(1, 1) : var == 2 ? k1_tab(2, 1) : var == 3 ? k1_tab(3, 1) : k1_tab(4, 1);
    const unsigned long long tb = wn ? t1 : t0;
#pragma unroll
    for (int i = 0; i < 10; ++i) {
      const unsigned code = (unsigned)(tb >> (5 * i)) & 31u;
      aB[i] = lb + (code >> 2) * 8192 + (((code & 3) ^ swz) * 32);
    }
    const int sa = (c >> 1) & 7;
    aA[0] = (64 * wm + c) * 128 + ((g ^ sa) * 16);
    aA[1] = aA[0] ^ 64;
  }

  {
    asm volatile("s_waitcnt vmcnt(13)" : "+v"(gx0), "+v"(gx1), "+v"(gw0), "+v"(gw1)::"memory");
    float s = 0.f;
#pragma unroll
    for (int e = 0; e < 4; ++e) {
      s = __builtin_fmaf(__uint_as_float(gx0[e] << 16), __uint_as_float(gw0[e] << 16), s);
      s = __builtin_fmaf(__uint_as_float(gx0[e] & 0xffff0000u), __uint_as_float(gw0[e] & 0xffff0000u), s);
      s = __builtin_fmaf(__uint_as_float(gx1[e] << 16), __uint_as_float(gw1[e] << 16), s);
      s = __builtin_fmaf(__uint_as_float(gx1[e] & 0xffff0000u), __uint_as_float(gw1[e] & 0xffff0000u), s);
    }
#pragma unroll
    for (int o = 1; o < 64; o <<= 1) s += __shfl_xor(s, o);
    const float gv = 1.0f / (1.0f + __expf(-s));
    if (lane == 0) {
      float* gp = gate + bid * 8 + wid;
      asm volatile("global_store_dword %0, %1, %2" ::"v"(0u), "v"(gv), "s"(gp) : "memory");
    }
  }

  f32x4 acc[10][4];
#pragma unroll
  for (int i = 0; i < 10; ++i)
#pragma unroll
    for (int m = 0; m < 4; ++m) acc[i][m] = (f32x4){0.f, 0.f, 0.f, 0.f};

  WAIT_V(9);
  BAR();
  if (grp == 0) k1_main<0>(lds, cx, aB, aA, acc); else k1_main<1>(lds, cx, aB, aA, acc);

  int tz = threadIdx.x;
  asm volatile("" : "+v"(tz));
  const int lane2 = tz & 63, g2 = lane2 >> 4;
  const int jj = cg - 24;
  if (bid == 0) flags[tz * 16] = 0u;
  {
    const unsigned lo = (unsigned)(row0 + 64 * wm + 16 * (g2 & 1) + (lane2 & 15)) * 32u + (g2 >> 1) * 16u;
    char* hp = act + (size_t)(ttype == 0 ? cg * 10 + 5 * wn : 220 + 2 * wn) * PANEL + lo;
    char* qp = qbuf + (size_t)(2 * (ttype == 1 ? 26 + 3 * wn : (ttype == 2 ? 16 + 5 * wn : 2 * jj + wn))) * PANEL + lo;
    const int qs0 = ttype == 1 ? 2 : (ttype == 2 ? 0 : 4);
#pragma unroll
    for (int s = 4; s >= 0; --s) {
      const bool isH = ttype == 0 || (ttype == 1 && s < 2);
      const bool isQ = ttype == 2 || (ttype == 1 && s >= 2) || (ttype == 3 && s == 4);
      if (isH) epi_h(acc[2 * s], acc[2 * s + 1], hp + (size_t)s * PANEL);
      else if (isQ) epi_q(acc[2 * s], acc[2 * s + 1], qp + (size_t)(2 * (s - qs0)) * PANEL);
    }
  }
  if (ttype == 3) {
    LAS char* red = lds + (wm * 4 + 2 * wn) * 8448;
    bf16x8 pk[4][4];
#pragma unroll
    for (int s = 0; s < 4; ++s)
#pragma unroll
      for (int m = 0; m < 4; ++m) { pk[s][m] = pack8(acc[2 * s][m], acc[2 * s + 1][m]); asm volatile("" : "+v"(pk[s][m])); }
    epi_kv(pk[0], pk[1], lds + K1_WPHI + (2 * wn) * 2048, red, lane2);
    epi_kv(pk[2], pk[3], lds + K1_WPHI + (2 * wn + 1) * 2048, red + 8448, lane2);
    __syncthreads();
    for (int e = tz; e < 4 * 528; e += 512) {
      LAS char* p = lds + e * 16;
      f32x4 s = *(LAS f32x4*)p;
      s += *(LAS f32x4*)(p + 4 * 8448);
      s += *(LAS f32x4*)(p + 8 * 8448);
      s += *(LAS f32x4*)(p + 12 * 8448);
      *(f32x4*)(kvp + ((size_t)rt * 32 + 4 * jj) * KVP + e * 4) = s;
    }
  }
}

constexpr int K2_ST = 32768, K2_NST = 4, K2_AUX = K2_ST * K2_NST, K2_LDS = K2_AUX + 8192;
constexpr int FL_OCNT = 0;
constexpr int FL_X = 16;
#define RLX_AGENT __ATOMIC_RELAXED, __HIP_MEMORY_SCOPE_AGENT

DI void glds_s1(unsigned voff, const void* sbase, unsigned ldsaddr) {
  asm volatile("s_mov_b32 m0, %2\n\ts_nop 0\n\tglobal_load_lds_dwordx4 %0, %1 sc1" ::"v"(voff), "s"(sbase), "s"(ldsaddr) : "memory");
}
DI void st16_sc1(void* p, u32x4 v) { asm volatile("global_store_dwordx4 %0, %1, off sc1\n\ts_nop 1" ::"v"(p), "v"(v) : "memory"); }
DI void wait_ge(const unsigned* p, unsigned need) {
  for (unsigned spins = 0; spins < (1u << 22); ++spins) {
    if ((unsigned)__builtin_amdgcn_readfirstlane(__hip_atomic_load(p, RLX_AGENT)) >= need) break;
    __builtin_amdgcn_s_sleep(1);
  }
}

DI f32x4 sel4(int cnd, f32x4 a, f32x4 b) { return cnd ? a : b; }

struct K2Ctx {
  const char* sA; const char* sB;
  unsigned voffB, vlin;
  unsigned ldsA, ldsB;
};
template <bool SC1> DI void k2_issue(K2Ctx& c, int slot) {
  const unsigned o = slot * K2_ST;
  if (SC1) { glds_s1(c.vlin, c.sA, c.ldsA + o); glds_s1(c.vlin, c.sA + 1024, c.ldsA + o + 1024); }
  else     { glds_s(c.vlin, c.sA, c.ldsA + o); glds_s(c.vlin, c.sA + 1024, c.ldsA + o + 1024); }
  glds_s(c.voffB, c.sB, c.ldsB + o);
  glds_s(c.voffB, c.sB + 128, c.ldsB + o + 8192);
  c.sA += 4 * PANEL;
  c.sB += 64 * 2048;
}
template <int SLOT> DI void k2_load(LAS char* lds, unsigned aA, const unsigned (&aB)[4], bf16x8 (&fb)[4], bf16x8 (&fa)[4]) {
#pragma unroll
  for (int m = 0; m < 4; ++m) fa[m] = *(LAS bf16x8*)(lds + aA + ((SLOT & 1) * K2_ST + m * 512));
#pragma unroll
  for (int i = 0; i < 4; ++i) fb[i] = tr_frag(lds + aB[i], (SLOT & 1) * K2_ST);
}
DI void k2_mma(f32x4 (&acc)[4][4], const bf16x8 (&fb)[4], const bf16x8 (&fa)[4]) {
  __builtin_amdgcn_s_setprio(1);
#pragma unroll
  for (int i = 0; i < 4; ++i)
#pragma unroll
    for (int m = 0; m < 4; ++m) MFMA_ACC(acc[i][m], fb[i], fa[m]);
  __builtin_amdgcn_s_setprio(0);
}
template <int SLOT, int ISSUE, int WAITN, bool LASTBAR>
DI void k2_step(LAS char* lds, K2Ctx& c, const unsigned (&aA)[2], const unsigned (&aB)[2][4], f32x4 (&acc)[4][4]) {
  bf16x8 fb[4], fa[4];
  k2_load<SLOT>(lds, aA[SLOT >> 1], aB[SLOT >> 1], fb, fa);
  if (ISSUE == 1) k2_issue<false>(c, (SLOT + 3) & 3);
  if (ISSUE == 2) k2_issue<true>(c, (SLOT + 3) & 3);
  asm volatile("s_waitcnt vmcnt(%0)" ::"i"(WAITN) : "memory");
  WAIT_L0();
  BAR();
  k2_mma(acc, fb, fa);
  if (LASTBAR) BAR();
}
template <int ISSUE> DI void k2_step4(LAS char* lds, K2Ctx& c, const unsigned (&aA)[2], const unsigned (&aB)[2][4], f32x4 (&acc)[4][4]) {
  k2_step<0, ISSUE, 8, true>(lds, c, aA, aB, acc);
  k2_step<1, ISSUE, 8, true>(lds, c, aA, aB, acc);
  k2_step<2, ISSUE, 8, true>(lds, c, aA, aB, acc);
  k2_step<3, ISSUE, 8, true>(lds, c, aA, aB, acc);
}

__global__ __launch_bounds__(512, 2) void k2_kernel(char* __restrict__ act, const char* __restrict__ qbuf,
                                                    const unsigned short* __restrict__ wphi, const char* __restrict__ wd,
                                                    const char* __restrict__ wo, const float* __restrict__ kvp,
                                                    const float* __restrict__ gate, char* __restrict__ xbuf,
                                                    unsigned* __restrict__ flags, unsigned short* __restrict__ out) {
  extern __shared__ __attribute__((aligned(16))) char smem[];
  LAS char* lds = (LAS char*)smem;
  const unsigned ldsb = (unsigned)(size_t)lds;
  const int tid = threadIdx.x, lane = tid & 63;
  const int wid = __builtin_amdgcn_readfirstlane(tid >> 6);
  const int grp = wid >> 2, j = wid & 3, wm = j >> 1, wn = j & 1;
  const int bid = blockIdx.x, xcd = bid & 7, idx = bid >> 3;
  const int ks = xcd >> 2, qd = xcd & 3;
  const int tm = (qd >> 1) * 8 + (idx & 7), tn = (qd & 1) * 4 + (idx >> 3);
  const int g = lane >> 4, c = lane & 15, q = (lane >> 2) & 3, p = lane & 3;

  K2Ctx cx;
  {
    const int r = lane >> 3, ch = lane & 7;
    const int kb = 2 * grp + (j >> 1);
    cx.sA = act + (size_t)(ks * 144 + kb) * PANEL + (tm * 128 + (j & 1) * 64) * 32;
    cx.ldsA = ldsb + kb * 4096 + (j & 1) * 2048;
    cx.vlin = lane * 16;
    const int kr = 32 * grp + 8 * j;
    cx.sB = wd + (size_t)(ks * 2304 + kr) * 2048 + tn * 256;
    cx.voffB = r * 2048 + (((ch >> 1) ^ (((r >> 1) & 1) | ((j & 1) << 1))) * 32) + (ch & 1) * 16;
    cx.ldsB = ldsb + 16384 + kr * 128;
  }
  k2_issue<false>(cx, 0); k2_issue<false>(cx, 1); k2_issue<false>(cx, 2);

  {
    const int head = bid & 31, rb = bid >> 5;
    LAS char* aux = lds + K2_AUX;
    bf16x8 qf[2];
    float gt[2];
#pragma unroll
    for (int m = 0; m < 2; ++m) {
      const int row = rb * 256 + wid * 32 + m * 16 + c;
      qf[m] = *(const bf16x8*)(qbuf + (size_t)(2 * head + (g >> 1)) * PANEL + row * 32 + (g & 1) * 16);
      gt[m] = gate[row];
    }
    for (int e = tid; e < 528; e += 512) {
      f32x4 s = {0.f, 0.f, 0.f, 0.f};
#pragma unroll
      for (int r = 0; r < 8; ++r) s += *(const f32x4*)(kvp + ((size_t)r * 32 + head) * KVP + e * 4);
      *(LAS u32x2*)(aux + e * 8) = pk4(s);
    }
    if (tid < 128) *(LAS u32x4*)(aux + 528 * 8 + tid * 16) = *(const u32x4*)((const char*)wphi + (size_t)head * 2048 + tid * 16);
    __syncthreads();
    bf16x8 wf[2];
#pragma unroll
    for (int fb = 0; fb < 2; ++fb) {
      LAS char* a = aux + 528 * 8 + (8 * g + q) * 64 + fb * 32 + p * 8;
      s16x4 lo = __builtin_amdgcn_ds_read_tr16_b64_v4i16((LAS s16x4*)a);
      s16x4 hi = __builtin_amdgcn_ds_read_tr16_b64_v4i16((LAS s16x4*)(a + 256));
      wf[fb] = __builtin_shufflevector(lo, hi, 0, 1, 2, 3, 4, 5, 6, 7);
    }
    bf16x8 kf[2][2], zf[2];
#pragma unroll
    for (int h = 0; h < 2; ++h) {
#pragma unroll
      for (int db = 0; db < 2; ++db) {
        const u32x2 lo = *(LAS u32x2*)(aux + (((2 * h) * 2 + db) * 64 + lane) * 8);
        const u32x2 hi = *(LAS u32x2*)(aux + (((2 * h + 1) * 2 + db) * 64 + lane) * 8);
        const u32x4 w = {lo[0], lo[1], hi[0], hi[1]};
        kf[h][db] = __builtin_bit_cast(bf16x8, w);
      }
      const u32x2 lo = *(LAS u32x2*)(aux + (512 + (2 * h) * 4 + g) * 8);
      const u32x2 hi = *(LAS u32x2*)(aux + (512 + (2 * h + 1) * 4 + g) * 8);
      const u32x4 w = {lo[0], lo[1], hi[0], hi[1]};
      zf[h] = __builtin_bit_cast(bf16x8, w);
    }
    const f32x4 Z = {0.f, 0.f, 0.f, 0.f};
    f32x4 pp[2][2];
#pragma unroll
    for (int m = 0; m < 2; ++m) { pp[m][0] = MFMA(wf[0], qf[m], Z); pp[m][1] = MFMA(wf[1], qf[m], Z); }
    bf16x8 fp[2], fn[2];
    feature_map2(pp, fp, fn);
    u32x2 w0[2], w1[2];
#pragma unroll
    for (int m = 0; m < 2; ++m) {
      f32x4 n0 = MFMA(kf[0][0], fp[m], Z); n0 = MFMA(kf[1][0], fn[m], n0);
      f32x4 n1 = MFMA(kf[0][1], fp[m], Z); n1 = MFMA(kf[1][1], fn[m], n1);
      f32x4 dn = MFMA(zf[0], fp[m], Z); dn = MFMA(zf[1], fn[m], dn);
      const float sc = gt[m] / (dn[0] + 1e-6f);
      w0[m] = pk4(n0 * sc); w1[m] = pk4(n1 * sc);
    }
    char* op = act + (size_t)(224 + 2 * head) * PANEL + (rb * 256 + wid * 32 + 16 * (g & 1) + c) * 32 + (g >> 1) * 16;
    st16_sc1(op, widen(w0[0], w0[1]));
    st16_sc1(op + PANEL, widen(w1[0], w1[1]));
    WAIT_V(0);
    __syncthreads();
    if (tid == 0) __hip_atomic_fetch_add(flags + (FL_OCNT + rb) * 16, 1u, RLX_AGENT);
  }

  unsigned aA[2], aB[2][4];
  {
    aA[0] = (2 * grp + (g >> 1)) * 4096 + (64 * wm + c) * 32 + (g & 1) * 16;
    aA[1] = aA[0] + 2 * K2_ST;
    const int swz = ((q >> 1) & 1) | ((g & 1) << 1);
#pragma unroll
    for (int i = 0; i < 4; ++i) {
      aB[0][i] = 16384 + wn * 8192 + (32 * grp + 8 * g + q) * 128 + p * 8 + ((i ^ swz) * 32);
      aB[1][i] = aB[0][i] + 2 * K2_ST;
    }
  }
  float og[4];
#pragma unroll
  for (int m = 0; m < 4; ++m) og[m] = 1.0f - gate[tm * 128 + 64 * wm + 16 * m + c];
  asm volatile("" : "+v"(og[0]), "+v"(og[1]), "+v"(og[2]), "+v"(og[3]));
  f32x4 acc[4][4];
#pragma unroll
  for (int i = 0; i < 4; ++i)
#pragma unroll
    for (int m = 0; m < 4; ++m) acc[i][m] = (f32x4){0.f, 0.f, 0.f, 0.f};

  if (grp == 1) BAR();
#pragma unroll 1
  for (int t = 0; t < 16; t += 4) k2_step4<1>(lds, cx, aA, aB, acc);
  k2_step<0, 1, 8, false>(lds, cx, aA, aB, acc);
  if (ks == 1) {
    if (wid == 0) {
      wait_ge(flags + (FL_OCNT + (tm >> 1)) * 16, 32u);
      __builtin_amdgcn_fence(__ATOMIC_ACQUIRE, "agent");
      WAIT_V(0);
    }
    cx.sB = wo + (32 * grp + 8 * j) * 2048 + tn * 256;
  }
  BAR();
  k2_step<1, 2, 8, true>(lds, cx, aA, aB, acc);
  k2_step<2, 2, 8, true>(lds, cx, aA, aB, acc);
  k2_step<3, 2, 8, true>(lds, cx, aA, aB, acc);
  if (ks == 1) {
    MFMA_DRAIN();
#pragma unroll
    for (int i = 0; i < 4; ++i)
#pragma unroll
      for (int m = 0; m < 4; ++m) acc[i][m] *= og[m];
    SCHED(); asm volatile("s_nop 1"); SCHED();
  }
#pragma unroll 1
  for (int t = 20; t < 32; t += 4) k2_step4<2>(lds, cx, aA, aB, acc);
  k2_step<0, 2, 8, true>(lds, cx, aA, aB, acc);
  k2_step<1, 0, 4, true>(lds, cx, aA, aB, acc);
  k2_step<2, 0, 0, true>(lds, cx, aA, aB, acc);
  k2_step<3, 0, 0, false>(lds, cx, aA, aB, acc);
  if (grp == 0) BAR();
  MFMA_DRAIN();
  if (ks == 0) {
#pragma unroll
    for (int i = 0; i < 4; ++i)
#pragma unroll
      for (int m = 0; m < 4; ++m) acc[i][m] *= og[m];
  }

  f32x4 mine[2][4];
  {
    LAS char* wr = lds + wid * 8192 + lane * 16;
    LAS char* rd = lds + (wid ^ 4) * 8192 + lane * 16;
#pragma unroll
    for (int i = 0; i < 2; ++i)
#pragma unroll
      for (int m = 0; m < 4; ++m) {
        *(LAS f32x4*)(wr + (i * 4 + m) * 1024) = sel4(grp, acc[i][m], acc[2 + i][m]);
        mine[i][m] = sel4(grp, acc[2 + i][m], acc[i][m]);
      }
    __syncthreads();
#pragma unroll
    for (int i = 0; i < 2; ++i)
#pragma unroll
      for (int m = 0; m < 4; ++m) mine[i][m] += *(LAS f32x4*)(rd + (i * 4 + m) * 1024);
  }
  {
    char* xs = xbuf + ((size_t)bid * 8 + wid) * 4096 + lane * 16;
#pragma unroll
    for (int i = 0; i < 2; ++i)
#pragma unroll
      for (int m = 0; m < 2; ++m)
        st16_sc1(xs + (i * 2 + m) * 1024, __builtin_bit_cast(u32x4, sel4(ks, mine[i][m], mine[i][2 + m])));
    WAIT_V(0);
    __syncthreads();
    if (tid == 0) __hip_atomic_store(flags + (FL_X + bid) * 16, 1u, RLX_AGENT);
    if (wid == 0) wait_ge(flags + (FL_X + (bid ^ 4)) * 16, 1u);
    __builtin_amdgcn_fence(__ATOMIC_ACQUIRE, "wavefront");
    __syncthreads();
    const char* xr = xbuf + ((size_t)(bid ^ 4) * 8 + wid) * 4096 + lane * 16;
    f32x4 o[4];
    asm volatile("global_load_dwordx4 %0, %4, off sc1\n\tglobal_load_dwordx4 %1, %4, off offset:1024 sc1\n\t"
                 "global_load_dwordx4 %2, %4, off offset:2048 sc1\n\tglobal_load_dwordx4 %3, %4, off offset:3072 sc1\n\t"
                 "s_waitcnt vmcnt(0)"
                 : "=&v"(o[0]), "=&v"(o[1]), "=&v"(o[2]), "=&v"(o[3]) : "v"(xr) : "memory");
#pragma unroll
    for (int m = 0; m < 2; ++m) {
      const f32x4 v0 = sel4(ks, mine[0][2 + m], mine[0][m]) + o[m];
      const f32x4 v1 = sel4(ks, mine[1][2 + m], mine[1][m]) + o[2 + m];
      const u32x4 w = widen(pk4(v0), pk4(v1));
      unsigned short* op = out + (size_t)(tm * 128 + 64 * wm + 32 * ks + 16 * m + c) * D_ + tn * 128 + 64 * wn + 32 * grp + 16 * (g & 1) + 8 * (g >> 1);
      *(u32x4*)op = w;
    }
  }
}

extern "C" void kernel_launch(void* const* d_in, const int* in_sizes, int n_in, void* d_out, int out_size, void* d_ws,
                              size_t ws_size, hipStream_t stream) {
  const unsigned short* x = (const unsigned short*)d_in[0];
  const unsigned short* wqkv = (const unsigned short*)d_in[1];
  const unsigned short* wphi = (const unsigned short*)d_in[2];
  const char* wo = (const char*)d_in[3];
  const unsigned short* wg = (const unsigned short*)d_in[5];
  const unsigned short* wu = (const unsigned short*)d_in[6];
  const char* wd = (const char*)d_in[7];
  const unsigned short* gatew = (const unsigned short*)d_in[8];
  char* ws = (char*)d_ws;
  char* act = ws;
  char* qbuf = ws + (18u << 20);
  float* kvp = (float*)(ws + (22u << 20));
  char* xbuf = ws + (25u << 20) ;
  unsigned* flags = (unsigned*)(ws + (33u << 20));
  float* gate = (float*)(ws + (34u << 20));
  (void)hipFuncSetAttribute((const void*)k1_kernel, hipFuncAttributeMaxDynamicSharedMemorySize, K1_LDS);
  (void)hipFuncSetAttribute((const void*)k2_kernel, hipFuncAttributeMaxDynamicSharedMemorySize, K2_LDS);
  k1_kernel<<<256, 512, K1_LDS, stream>>>(x, wqkv, wphi, wg, wu, gatew, act, qbuf, kvp, gate, flags);
  k2_kernel<<<256, 512, K2_LDS, stream>>>(act, qbuf, wphi, wd, wo, kvp, gate, xbuf, flags, (unsigned short*)d_out);
}
```

```cpp
#include <hip/hip_runtime.h>
#include <hip/hip_bf16.h>
#include <stdint.h>

typedef short bf16x8 __attribute__((ext_vector_type(8)));
typedef short s16x4 __attribute__((ext_vector_type(4)));
typedef float f32x4 __attribute__((ext_vector_type(4)));
typedef float f32x2 __attribute__((ext_vector_type(2)));
typedef unsigned u32x4 __attribute__((ext_vector_type(4)));
typedef unsigned u32x2 __attribute__((ext_vector_type(2)));
typedef __bf16 bf16x2_t __attribute__((ext_vector_type(2)));
#define LAS __attribute__((address_space(3)))
#define GAS __attribute__((address_space(1)))
#define DI __device__ __forceinline__

constexpr int S_ = 2048, D_ = 1024, F_ = 3584;
constexpr int PANEL = S_ * 32;
constexpr int KVP = 2112;

#define STAMP_DECL
#define STAMP(i)
#define STAMP_OUT(base, tag)
DI void glds_s(unsigned voff, const void* sbase, unsigned ldsaddr) {
  asm volatile("s_mov_b32 m0, %2\n\ts_nop 0\n\tglobal_load_lds_dwordx4 %0, %1" ::"v"(voff), "s"(sbase), "s"(ldsaddr) : "memory");
}
DI void glds_v(const void* vaddr, unsigned ldsaddr) {
  asm volatile("s_mov_b32 m0, %1\n\ts_nop 0\n\tglobal_load_lds_dwordx4 %0, off" ::"v"(vaddr), "s"(ldsaddr) : "memory");
}
DI const char* uni_ptr(const char* p) {
  const unsigned long long v = (unsigned long long)p;
  const unsigned lo = __builtin_amdgcn_readfirstlane((unsigned)v), hi = __builtin_amdgcn_readfirstlane((unsigned)(v >> 32));
  return (const char*)(((unsigned long long)hi << 32) | lo);
}
#define WAIT_V(n) asm volatile("s_waitcnt vmcnt(" #n ")" ::: "memory")
#define WAIT_L0() __builtin_amdgcn_s_waitcnt(0xC07F)
#define SCHED() __builtin_amdgcn_sched_barrier(0)
#define BAR() do { SCHED(); asm volatile("" ::: "memory"); __builtin_amdgcn_s_barrier(); asm volatile("" ::: "memory"); SCHED(); } while (0)
#define MFMA_ACC(c, a, b) asm volatile("v_mfma_f32_16x16x32_bf16 %0, %1, %2, %0" : "+v"(c) : "v"(a), "v"(b))
#define MFMA_DRAIN() do { SCHED(); asm volatile("s_nop 7\n\ts_nop 7" ::: "memory"); SCHED(); } while (0)
#define MFMA(a, b, c) __builtin_amdgcn_mfma_f32_16x16x32_bf16((a), (b), (c), 0, 0, 0)

DI unsigned pk2(float lo, float hi) {
  f32x2 v = {lo, hi};
  bf16x2_t r = __builtin_convertvector(v, bf16x2_t);
  return __builtin_bit_cast(unsigned, r);
}
DI bf16x8 pack8(const f32x4& a, const f32x4& b) {
  u32x4 p = {pk2(a[0], a[1]), pk2(a[2], a[3]), pk2(b[0], b[1]), pk2(b[2], b[3])};
  return __builtin_bit_cast(bf16x8, p);
}
DI bf16x8 tr_frag(LAS char* p, int off) {
  s16x4 a = __builtin_amdgcn_ds_read_tr16_b64_v4i16((LAS s16x4*)(p + off));
  s16x4 b = __builtin_amdgcn_ds_read_tr16_b64_v4i16((LAS s16x4*)(p + off + 512));
  return __builtin_shufflevector(a, b, 0, 1, 2, 3, 4, 5, 6, 7);
}
DI float bf2f(unsigned short u) { return __uint_as_float(((unsigned)u) << 16); }
DI float gelu_mul(float x, float u) {
  const float c0 = -2.3022081984f;
  const float c1 = -2.3022081984f * 0.044715f;
  float t = x * x;
  float p = __builtin_fmaf(t, c1, c0);
  float e = __builtin_amdgcn_exp2f(x * p);
  float r = __builtin_amdgcn_rcpf(1.0f + e);
  return x * r * u;
}
DI void swap16(float& a, float& b) {
  auto r = __builtin_amdgcn_permlane16_swap(__float_as_uint(a), __float_as_uint(b), false, false);
  a = __uint_as_float(r[0]); b = __uint_as_float(r[1]);
}
DI void swap32(float& a, float& b) {
  auto r = __builtin_amdgcn_permlane32_swap(__float_as_uint(a), __float_as_uint(b), false, false);
  a = __uint_as_float(r[0]); b = __uint_as_float(r[1]);
}
template <bool MAX> DI float rop(float a, float b) { return MAX ? fmaxf(a, b) : a + b; }
template <bool MAX> DI void xrow4(float& a, float& b, float& c, float& d) {
  swap32(a, b); float r1 = rop<MAX>(a, b);
  swap32(c, d); float r2 = rop<MAX>(c, d);
  swap16(r1, r2); float r = rop<MAX>(r1, r2);
  float u = r, v = r; swap16(u, v);
  a = u; b = u; swap32(a, b);
  c = v; d = v; swap32(c, d);
}
DI u32x4 widen(u32x2 a, u32x2 b) {
  auto r0 = __builtin_amdgcn_permlane16_swap(a[0], b[0], false, false);
  auto r1 = __builtin_amdgcn_permlane16_swap(a[1], b[1], false, false);
  u32x4 w = {r0[0], r1[0], r0[1], r1[1]};
  return w;
}
DI u32x2 pk4(const f32x4& v) { u32x2 w = {pk2(v[0], v[1]), pk2(v[2], v[3])}; return w; }

DI void feature_map2(const f32x4 (&p)[2][2], bf16x8 (&fpos)[2], bf16x8 (&fneg)[2]) {
  const float L2E = 1.4426950408889634f;
  float mx[2], nm[2];
#pragma unroll
  for (int t = 0; t < 2; ++t) {
    const f32x4 &p0 = p[t][0], &p1 = p[t][1];
    mx[t] = fmaxf(fmaxf(fmaxf(p0[0], p0[1]), fmaxf(p0[2], p0[3])), fmaxf(fmaxf(p1[0], p1[1]), fmaxf(p1[2], p1[3])));
    nm[t] = -fminf(fminf(fminf(p0[0], p0[1]), fminf(p0[2], p0[3])), fminf(fminf(p1[0], p1[1]), fminf(p1[2], p1[3])));
  }
  xrow4<true>(mx[0], nm[0], mx[1], nm[1]);
  f32x4 e[2][2], n[2][2];
  float sp[2], sn[2];
#pragma unroll
  for (int t = 0; t < 2; ++t) {
    const float a = -mx[t] * L2E, b = -nm[t] * L2E;
#pragma unroll
    for (int h = 0; h < 2; ++h)
#pragma unroll
      for (int r = 0; r < 4; ++r) {
        e[t][h][r] = __builtin_amdgcn_exp2f(__builtin_fmaf(p[t][h][r], L2E, a));
        n[t][h][r] = __builtin_amdgcn_exp2f(__builtin_fmaf(p[t][h][r], -L2E, b));
      }
    sp[t] = ((e[t][0][0] + e[t][0][1]) + (e[t][0][2] + e[t][0][3])) + ((e[t][1][0] + e[t][1][1]) + (e[t][1][2] + e[t][1][3]));
    sn[t] = ((n[t][0][0] + n[t][0][1]) + (n[t][0][2] + n[t][0][3])) + ((n[t][1][0] + n[t][1][1]) + (n[t][1][2] + n[t][1][3]));
  }
  xrow4<false>(sp[0], sn[0], sp[1], sn[1]);
#pragma unroll
  for (int t = 0; t < 2; ++t) {
    const float rp = __builtin_amdgcn_rcpf(sp[t]), rn = __builtin_amdgcn_rcpf(sn[t]);
    fpos[t] = pack8(e[t][0] * rp, e[t][1] * rp);
    fneg[t] = pack8(n[t][0] * rn, n[t][1] * rn);
  }
}

constexpr int K1_A0 = 0, K1_ABUF = 32768, K1_B0 = 65536, K1_BBUF = 40960, K1_WPHI = 147456, K1_DUMP = 147456 + 8192, K1_LDS = 147456 + 8192 + 2048;

constexpr unsigned long long k1_tab(int var, int wn) {
  unsigned long long t = 0;
  for (int i = 0; i < 10; ++i) {
    const int s = i >> 1, half = i & 1;
    int panel = 0, c32 = 0;
    if (var <= 1) {
      const int b = 5 * wn + s;
      if (var == 1) { if (b < 2) { panel = 4; c32 = b + 2 * half; } else { panel = ((b - 2) >> 2) + 2 * half; c32 = (b - 2) & 3; } }
      else          { if (b >= 8) { panel = 4; c32 = b - 8 + 2 * half; } else { panel = (b >> 2) + 2 * half; c32 = b & 3; } }
    } else if (var == 2) {
      if (s < 2) { panel = half; c32 = 2 * wn + s; } else { const int u = 3 * wn + s - 2; panel = 2 + (u >> 1); c32 = 2 * (u & 1) + half; }
    } else if (var == 3) {
      const int u = 5 * wn + s; panel = u >> 1; c32 = 2 * (u & 1) + half;
    } else {
      if (s == 4) { panel = 4; c32 = 2 * wn + half; } else { panel = (s & 1) * 2 + wn; c32 = (s >> 1) * 2 + half; }
    }
    t |= (unsigned long long)(panel * 4 + c32) << (5 * i);
  }
  return t;
}

struct K1Ctx {
  const char* sA;
  const char* sB[4];
  unsigned stepB[4];
  const char* vB4;
  unsigned stepB4;
  unsigned voffA0, voffA1, voffB[4];
  unsigned ldsA, ldsB;
};

template <int BUF> DI void k1_issue_A(K1Ctx& c) {
#pragma unroll
  for (int i = 0; i < 4; ++i) glds_s((i & 1) ? c.voffA1 : c.voffA0, c.sA + i * 16384, c.ldsA + BUF * K1_ABUF + i * 1024);
  c.sA += 128;
}
template <int BUF> DI void k1_issue_B(K1Ctx& c) {
#pragma unroll
  for (int i = 0; i < 4; ++i) { glds_s(c.voffB[i], c.sB[i], c.ldsB + BUF * K1_BBUF + i * 8192); c.sB[i] += c.stepB[i]; }
  glds_v(c.vB4, c.ldsB + BUF * K1_BBUF + 4 * 8192); c.vB4 += c.stepB4;
}

template <int BUF, int KS> DI void k1_load(LAS char* lds, const unsigned (&aB)[10], const unsigned (&aA)[2], bf16x8 (&fb)[10], bf16x8 (&fa)[4]) {
#pragma unroll
  for (int m = 0; m < 4; ++m) fa[m] = *(LAS bf16x8*)(lds + aA[KS] + (K1_A0 + BUF * K1_ABUF + m * 2048));
#pragma unroll
  for (int i = 0; i < 10; ++i) fb[i] = tr_frag(lds + aB[i], BUF * K1_BBUF + KS * 4096);
}
DI void k1_mma(f32x4 (&acc)[10][4], const bf16x8 (&fb)[10], const bf16x8 (&fa)[4]) {
  __builtin_amdgcn_s_setprio(1);
#pragma unroll
  for (int i = 0; i < 10; ++i)
#pragma unroll
    for (int m = 0; m < 4; ++m) MFMA_ACC(acc[i][m], fb[i], fa[m]);
  __builtin_amdgcn_s_setprio(0);
}
template <int GRP, int BUF, int MODE>
DI void k1_tile(LAS char* lds, K1Ctx& c, const unsigned (&aB)[10], const unsigned (&aA)[2], f32x4 (&acc)[10][4]) {
  bf16x8 fb[10], fa[4];
  k1_load<BUF, 0>(lds, aB, aA, fb, fa);
  if (MODE < 2) { if (GRP == 0) k1_issue_A<BUF ^ 1>(c); else k1_issue_B<BUF ^ 1>(c); WAIT_V(9); } else { WAIT_V(0); }
  WAIT_L0();
  BAR();
  k1_mma(acc, fb, fa);
  BAR();
  k1_load<BUF, 1>(lds, aB, aA, fb, fa);
  if (MODE == 0) { if (GRP == 0) k1_issue_B<BUF>(c); else k1_issue_A<BUF>(c); WAIT_V(9); }
  else if (MODE == 1) { WAIT_V(4); }
  WAIT_L0();
  BAR();
  k1_mma(acc, fb, fa);
  if (!(MODE == 2 && GRP == 1)) BAR();
}
template <int GRP>
DI void k1_main(LAS char* lds, K1Ctx& c, const unsigned (&aB)[10], const unsigned (&aA)[2], f32x4 (&acc)[10][4]) {
  if (GRP == 1) BAR();
#pragma unroll 1
  for (int t = 0; t < 14; t += 2) {
    k1_tile<GRP, 0, 0>(lds, c, aB, aA, acc);
    k1_tile<GRP, 1, 0>(lds, c, aB, aA, acc);
  }
  k1_tile<GRP, 0, 1>(lds, c, aB, aA, acc);
  k1_tile<GRP, 1, 2>(lds, c, aB, aA, acc);
  MFMA_DRAIN();
}

DI void epi_h(const f32x4 (&ga)[4], const f32x4 (&ua)[4], char* pp) {
#pragma unroll
  for (int mp = 0; mp < 2; ++mp) {
    u32x2 w[2];
#pragma unroll
    for (int h = 0; h < 2; ++h) {
      const int m = 2 * mp + h;
      w[h][0] = pk2(gelu_mul(ga[m][0], ua[m][0]), gelu_mul(ga[m][1], ua[m][1]));
      w[h][1] = pk2(gelu_mul(ga[m][2], ua[m][2]), gelu_mul(ga[m][3], ua[m][3]));
    }
    *(u32x4*)(pp + mp * 1024) = widen(w[0], w[1]);
  }
}
DI void epi_q(const f32x4 (&a0)[4], const f32x4 (&a1)[4], char* pp) {
#pragma unroll
  for (int mp = 0; mp < 2; ++mp) {
    *(u32x4*)(pp + mp * 1024) = widen(pk4(a0[2 * mp]), pk4(a0[2 * mp + 1]));
    *(u32x4*)(pp + PANEL + mp * 1024) = widen(pk4(a1[2 * mp]), pk4(a1[2 * mp + 1]));
  }
}
DI void epi_kv(const bf16x8 (&kp)[4], const bf16x8 (&vp)[4],
               LAS char* wphi  , LAS char* red  , int lane) {
  const int g = lane >> 4, c = lane & 15, q = (lane >> 2) & 3, p = lane & 3;
  bf16x8 wf[2];
#pragma unroll
  for (int fb = 0; fb < 2; ++fb) {
    LAS char* a = wphi + (4 * g + q) * 64 + fb * 32 + p * 8;
    s16x4 lo = __builtin_amdgcn_ds_read_tr16_b64_v4i16((LAS s16x4*)a);
    s16x4 hi = __builtin_amdgcn_ds_read_tr16_b64_v4i16((LAS s16x4*)(a + 1024));
    wf[fb] = __builtin_shufflevector(lo, hi, 0, 1, 2, 3, 4, 5, 6, 7);
  }
  const bool hit = (c >> 2) == g;
  const unsigned one_lo = hit ? ((c & 1) ? 0x3F800000u : 0x00003F80u) : 0u;
  const unsigned w0 = (c & 2) ? 0u : one_lo, w1 = (c & 2) ? one_lo : 0u;
  const u32x4 i0 = {w0, w1, 0u, 0u}, i1 = {0u, 0u, w0, w1};
  const bf16x8 I0 = __builtin_bit_cast(bf16x8, i0), I1 = __builtin_bit_cast(bf16x8, i1);
  const u32x4 onesu = {0x3F803F80u, 0x3F803F80u, 0x3F803F80u, 0x3F803F80u};
  const bf16x8 ONES = __builtin_bit_cast(bf16x8, onesu);
  const f32x4 Z = {0.f, 0.f, 0.f, 0.f};
  f32x4 kv[4][3];
#pragma unroll
  for (int a = 0; a < 4; ++a)
#pragma unroll
    for (int b = 0; b < 3; ++b) kv[a][b] = Z;
#pragma unroll
  for (int mp = 0; mp < 2; ++mp) {
    f32x4 zp[2][4], zv[2][2], pp[2][2];
#pragma unroll
    for (int h = 0; h < 2; ++h) { pp[h][0] = MFMA(wf[0], kp[2 * mp + h], Z); pp[h][1] = MFMA(wf[1], kp[2 * mp + h], Z); }
#pragma unroll
    for (int h = 0; h < 2; ++h) { zv[h][0] = MFMA(vp[2 * mp + h], I0, Z); zv[h][1] = MFMA(vp[2 * mp + h], I1, Z); }
    bf16x8 fp[2], fn[2];
    feature_map2(pp, fp, fn);
#pragma unroll
    for (int h = 0; h < 2; ++h) {
      zp[h][0] = MFMA(fp[h], I0, Z); zp[h][1] = MFMA(fp[h], I1, Z);
      zp[h][2] = MFMA(fn[h], I0, Z); zp[h][3] = MFMA(fn[h], I1, Z);
    }
    const bf16x8 vb0 = pack8(zv[0][0], zv[1][0]), vb1 = pack8(zv[0][1], zv[1][1]);
#pragma unroll
    for (int a = 0; a < 4; ++a) {
      const bf16x8 pa = pack8(zp[0][a], zp[1][a]);
      kv[a][0] = MFMA(pa, vb0, kv[a][0]);
      kv[a][1] = MFMA(pa, vb1, kv[a][1]);
      kv[a][2] = MFMA(pa, ONES, kv[a][2]);
    }
  }
#pragma unroll
  for (int a = 0; a < 4; ++a) {
    *(LAS f32x4*)(red + (a * 2 + 0) * 1024 + lane * 16) = kv[a][0];
    *(LAS f32x4*)(red + (a * 2 + 1) * 1024 + lane * 16) = kv[a][1];
    if (c == 0) *(LAS f32x4*)(red + 8192 + a * 64 + g * 16) = kv[a][2];
  }
}

__global__ __launch_bounds__(512, 2) void k1_kernel(const unsigned short* __restrict__ x, const unsigned short* __restrict__ wqkv,
                                                    const unsigned short* __restrict__ wphi, const unsigned short* __restrict__ wg,
                                                    const unsigned short* __restrict__ wu, const unsigned short* __restrict__ gatew,
                                                    char* __restrict__ act, char* __restrict__ qbuf,
                                                    float* __restrict__ kvp, float* __restrict__ gate, unsigned* __restrict__ flags,
                                                    const char* __restrict__ wd, const char* __restrict__ wo) {
  extern __shared__ __attribute__((aligned(16))) char smem[];
  LAS char* lds = (LAS char*)smem;
  const unsigned ldsb = (unsigned)(size_t)lds;
  const int tid = threadIdx.x, lane = tid & 63;
  const int wid = __builtin_amdgcn_readfirstlane(tid >> 6);
  const int grp = wid >> 2, j = wid & 3, wm = 2 * grp + (j >> 1), wn = j & 1;
  const int bid = blockIdx.x, xcd = bid & 7, idx = bid >> 3;
  const int rt = idx & 7, cg = xcd * 4 + (idx >> 3);
  const int row0 = rt * 256;
  const int g = lane >> 4, c = lane & 15;

  STAMP_DECL;
  u32x4 gx0, gx1, gw0, gw1;
  {
    const char* xr = (const char*)x + (size_t)(bid * 8 + wid) * 2048;
    const unsigned vo = lane * 16;
    asm volatile("global_load_dwordx4 %0, %4, %5\n\tglobal_load_dwordx4 %1, %4, %5 offset:1024\n\t"
                 "global_load_dwordx4 %2, %4, %6\n\tglobal_load_dwordx4 %3, %4, %6 offset:1024"
                 : "=&v"(gx0), "=&v"(gx1), "=&v"(gw0), "=&v"(gw1) : "v"(vo), "s"(xr), "s"(gatew) : "memory");
  }

  {
    const int id = wid * 256 + bid;
    if (id < 1152) {
      const char* pf = id < 896 ? wd + (size_t)id * 8192 : wo + (size_t)(id - 896) * 8192;
      asm volatile("s_mov_b32 m0, %2\n\ts_nop 0\n\tglobal_load_lds_dword %0, %1" ::"v"(lane * 128u), "s"(pf), "s"(ldsb + K1_DUMP + wid * 256) : "memory");
    }
  }

  const int ttype = cg < 22 ? 0 : (cg == 22 ? 1 : (cg == 23 ? 2 : 3));
  const unsigned SW = F_ * 2, SQ = 3 * D_ * 2;
  const char* pb[4]; unsigned ps[4]; const char* n0; const char* n1; unsigned ns;
  {
    const int odd = cg & 1, jj = cg - 24;
    const int wc = cg * 160 + (odd ? 32 : 0), nc = cg * 160 + (odd ? 0 : 128);
    const char* G = (const char*)wg; const char* U = (const char*)wu; const char* Q = (const char*)wqkv;
    const int o0 = ttype == 0 ? wc : (ttype == 1 ? 3520 : (ttype == 2 ? 512 : 1024 + 128 * jj));
    const int o1 = ttype == 0 ? wc + 64 : (ttype == 1 ? 3520 : (ttype == 2 ? 576 : 1088 + 128 * jj));
    const int o2 = ttype == 0 ? wc : (ttype == 1 ? 832 : (ttype == 2 ? 640 : 2048 + 128 * jj));
    const int o3 = ttype == 0 ? wc + 64 : (ttype == 1 ? 896 : (ttype == 2 ? 704 : 2112 + 128 * jj));
    const int o4 = ttype == 0 ? nc : (ttype == 1 ? 960 : (ttype == 2 ? 768 : 64 * jj));
    const int o5 = ttype == 0 ? nc : o4 + 32;
    pb[0] = (ttype <= 1 ? G : Q) + 2 * o0;
    pb[1] = (ttype == 0 ? G : (ttype == 1 ? U : Q)) + 2 * o1;
    pb[2] = (ttype == 0 ? U : Q) + 2 * o2;
    pb[3] = (ttype == 0 ? U : Q) + 2 * o3;
    n0 = (ttype == 0 ? G : Q) + 2 * o4;
    n1 = (ttype == 0 ? U : Q) + 2 * o5;
    ps[0] = ps[1] = ttype <= 1 ? SW : SQ;
    ps[2] = ps[3] = ns = ttype == 0 ? SW : SQ;
  }

  K1Ctx cx;
  {
    const int ar = (grp == 0 ? 128 : 0) + 32 * j;
    cx.sA = uni_ptr((const char*)x + (unsigned)(row0 + ar) * 2048u);
    cx.ldsA = ldsb + K1_A0 + ar * 128;
    const int r = lane >> 3, ch = lane & 7;
    cx.voffA0 = r * 2048 + ((ch ^ (r >> 1)) * 16);
    cx.voffA1 = cx.voffA0 ^ 64;
    const int kr = grp * 32 + 8 * j;
    const int swz = ((r >> 1) & 1) | ((j & 1) << 1);
    const int l32 = (ch >> 1) ^ swz;
    const unsigned coff = l32 * 32 + (ch & 1) * 16;
#pragma unroll
    for (int i = 0; i < 4; ++i) { cx.sB[i] = uni_ptr(pb[i] + (unsigned)kr * ps[i]); cx.stepB[i] = 64 * ps[i]; cx.voffB[i] = r * ps[i] + coff; }
    cx.vB4 = ((l32 & 2) ? n1 : n0) + (size_t)(kr + r) * ns + (coff & 63);
    cx.stepB4 = 64 * ns;
    cx.ldsB = ldsb + K1_B0 + kr * 128;
    asm volatile("s_nop 4" : "+s"(cx.sA), "+s"(cx.sB[0]), "+s"(cx.sB[1]), "+s"(cx.sB[2]), "+s"(cx.sB[3]));
  }
  if (ttype == 3) {
    glds_s(lane * 16, (const char*)wphi + (size_t)(cg - 24) * 8192 + wid * 1024, ldsb + K1_WPHI + wid * 1024);
  }
  if (grp == 0) { k1_issue_B<0>(cx); k1_issue_A<0>(cx); k1_issue_B<1>(cx); }
  else          { k1_issue_A<0>(cx); k1_issue_B<0>(cx); k1_issue_A<1>(cx); }

  unsigned aB[10], aA[2];
  {
    const int q = (lane >> 2) & 3, p = lane & 3;
    const int swz = ((q >> 1) & 1) | ((g & 1) << 1);
    const unsigned lb = K1_B0 + (8 * g + q) * 128 + p * 8;
    const int var = ttype == 0 ? (cg & 1) : ttype + 1;
    const unsigned long long t0 = var == 0 ? k1_tab(0, 0) : var == 1 ? k1_tab(1, 0) : var == 2 ? k1_tab(2, 0) : var == 3 ? k1_tab(3, 0) : k1_tab(4, 0);
    const unsigned long long t1 = var == 0 ? k1_tab(0, 1) : var == 1 ? k1_tab(1, 1) : var == 2 ? k1_tab(2, 1) : var == 3 ? k1_tab(3, 1) : k1_tab(4, 1);
    const unsigned long long tb = wn ? t1 : t0;
#pragma unroll
    for (int i = 0; i < 10; ++i) {
      const unsigned code = (unsigned)(tb >> (5 * i)) & 31u;
      aB[i] = lb + (code >> 2) * 8192 + (((code & 3) ^ swz) * 32);
    }
    const int sa = (c >> 1) & 7;
    aA[0] = (64 * wm + c) * 128 + ((g ^ sa) * 16);
    aA[1] = aA[0] ^ 64;
  }

  {
    asm volatile("s_waitcnt vmcnt(13)" : "+v"(gx0), "+v"(gx1), "+v"(gw0), "+v"(gw1)::"memory");
    float s = 0.f;
#pragma unroll
    for (int e = 0; e < 4; ++e) {
      s = __builtin_fmaf(__uint_as_float(gx0[e] << 16), __uint_as_float(gw0[e] << 16), s);
      s = __builtin_fmaf(__uint_as_float(gx0[e] & 0xffff0000u), __uint_as_float(gw0[e] & 0xffff0000u), s);
      s = __builtin_fmaf(__uint_as_float(gx1[e] << 16), __uint_as_float(gw1[e] << 16), s);
      s = __builtin_fmaf(__uint_as_float(gx1[e] & 0xffff0000u), __uint_as_float(gw1[e] & 0xffff0000u), s);
    }
#pragma unroll
    for (int o = 1; o < 64; o <<= 1) s += __shfl_xor(s, o);
    const float gv = 1.0f / (1.0f + __expf(-s));
    if (lane == 0) {
      float* gp = gate + bid * 8 + wid;
      asm volatile("global_store_dword %0, %1, %2" ::"v"(0u), "v"(gv), "s"(gp) : "memory");
    }
  }

  f32x4 acc[10][4];
#pragma unroll
  for (int i = 0; i < 10; ++i)
#pragma unroll
    for (int m = 0; m < 4; ++m) acc[i][m] = (f32x4){0.f, 0.f, 0.f, 0.f};

  WAIT_V(9);
  BAR();
  STAMP(1);
  if (grp == 0) k1_main<0>(lds, cx, aB, aA, acc); else k1_main<1>(lds, cx, aB, aA, acc);

  STAMP(2);
  int tz = threadIdx.x;
  asm volatile("" : "+v"(tz));
  const int lane2 = tz & 63, g2 = lane2 >> 4;
  const int jj = cg - 24;
  if (bid == 0) flags[tz * 16] = 0u;
  {
    const unsigned lo = (unsigned)(row0 + 64 * wm + 16 * (g2 & 1) + (lane2 & 15)) * 32u + (g2 >> 1) * 16u;
    char* hp = act + (size_t)(ttype == 0 ? cg * 10 + 5 * wn : 220 + 2 * wn) * PANEL + lo;
    char* qp = qbuf + (size_t)(2 * (ttype == 1 ? 26 + 3 * wn : (ttype == 2 ? 16 + 5 * wn : 2 * jj + wn))) * PANEL + lo;
    const int qs0 = ttype == 1 ? 2 : (ttype == 2 ? 0 : 4);
#pragma unroll
    for (int s = 4; s >= 0; --s) {
      const bool isH = ttype == 0 || (ttype == 1 && s < 2);
      const bool isQ = ttype == 2 || (ttype == 1 && s >= 2) || (ttype == 3 && s == 4);
      if (isH) epi_h(acc[2 * s], acc[2 * s + 1], hp + (size_t)s * PANEL);
      else if (isQ) epi_q(acc[2 * s], acc[2 * s + 1], qp + (size_t)(2 * (s - qs0)) * PANEL);
    }
  }
  STAMP(3);
  if (ttype == 3) {
    LAS char* red = lds + (wm * 4 + 2 * wn) * 8448;
    bf16x8 pk[4][4];
#pragma unroll
    for (int s = 0; s < 4; ++s)
#pragma unroll
      for (int m = 0; m < 4; ++m) { pk[s][m] = pack8(acc[2 * s][m], acc[2 * s + 1][m]); asm volatile("" : "+v"(pk[s][m])); }
    epi_kv(pk[0], pk[1], lds + K1_WPHI + (2 * wn) * 2048, red, lane2);
    epi_kv(pk[2], pk[3], lds + K1_WPHI + (2 * wn + 1) * 2048, red + 8448, lane2);
    STAMP(4);
    __syncthreads();
    STAMP(5);
    for (int e = tz; e < 4 * 528; e += 512) {
      LAS char* p = lds + e * 16;
      f32x4 s = *(LAS f32x4*)p;
      s += *(LAS f32x4*)(p + 4 * 8448);
      s += *(LAS f32x4*)(p + 8 * 8448);
      s += *(LAS f32x4*)(p + 12 * 8448);
      *(f32x4*)(kvp + ((size_t)rt * 32 + 4 * jj) * KVP + e * 4) = s;
    }
  }
  STAMP(6);
  STAMP_OUT((char*)gate + (1u << 20), ttype);
}

constexpr int K2_ST = 32768, K2_NST = 4, K2_AUX = K2_ST * K2_NST, K2_LDS = K2_AUX + 8192;
constexpr int FL_OCNT = 0;
constexpr int FL_X = 16;
#define RLX_AGENT __ATOMIC_RELAXED, __HIP_MEMORY_SCOPE_AGENT

DI void glds_s1(unsigned voff, const void* sbase, unsigned ldsaddr) {
  asm volatile("s_mov_b32 m0, %2\n\ts_nop 0\n\tglobal_load_lds_dwordx4 %0, %1 sc1" ::"v"(voff), "s"(sbase), "s"(ldsaddr) : "memory");
}
DI void st16_sc1(void* p, u32x4 v) { asm volatile("global_store_dwordx4 %0, %1, off sc1\n\ts_nop 1" ::"v"(p), "v"(v) : "memory"); }
DI void wait_ge(const unsigned* p, unsigned need) {
  for (unsigned spins = 0; spins < (1u << 22); ++spins) {
    if ((unsigned)__builtin_amdgcn_readfirstlane(__hip_atomic_load(p, RLX_AGENT)) >= need) break;
    __builtin_amdgcn_s_sleep(1);
  }
}

DI f32x4 sel4(int cnd, f32x4 a, f32x4 b) { return cnd ? a : b; }

struct K2Ctx {
  const char* sA; const char* sB;
  unsigned voffB, vlin;
  unsigned ldsA, ldsB;
};
template <bool SC1> DI void k2_issue(K2Ctx& c, int slot) {
  const unsigned o = slot * K2_ST;
  if (SC1) { glds_s1(c.vlin, c.sA, c.ldsA + o); glds_s1(c.vlin, c.sA + 1024, c.ldsA + o + 1024); }
  else     { glds_s(c.vlin, c.sA, c.ldsA + o); glds_s(c.vlin, c.sA + 1024, c.ldsA + o + 1024); }
  glds_s(c.voffB, c.sB, c.ldsB + o);
  glds_s(c.voffB, c.sB + 128, c.ldsB + o + 8192);
  c.sA += 4 * PANEL;
  c.sB += 64 * 2048;
}
template <int SLOT> DI void k2_load(LAS char* lds, unsigned aA, const unsigned (&aB)[4], bf16x8 (&fb)[4], bf16x8 (&fa)[4]) {
#pragma unroll
  for (int m = 0; m < 4; ++m) fa[m] = *(LAS bf16x8*)(lds + aA + ((SLOT & 1) * K2_ST + m * 512));
#pragma unroll
  for (int i = 0; i < 4; ++i) fb[i] = tr_frag(lds + aB[i], (SLOT & 1) * K2_ST);
}
DI void k2_mma(f32x4 (&acc)[4][4], const bf16x8 (&fb)[4], const bf16x8 (&fa)[4]) {
  __builtin_amdgcn_s_setprio(1);
#pragma unroll
  for (int i = 0; i < 4; ++i)
#pragma unroll
    for (int m = 0; m < 4; ++m) MFMA_ACC(acc[i][m], fb[i], fa[m]);
  __builtin_amdgcn_s_setprio(0);
}
template <int SLOT, int ISSUE, int WAITN, bool LASTBAR>
DI void k2_step(LAS char* lds, K2Ctx& c, const unsigned (&aA)[2], const unsigned (&aB)[2][4], f32x4 (&acc)[4][4]) {
  bf16x8 fb[4], fa[4];
  k2_load<SLOT>(lds, aA[SLOT >> 1], aB[SLOT >> 1], fb, fa);
  if (ISSUE == 1) k2_issue<false>(c, (SLOT + 3) & 3);
  if (ISSUE == 2) k2_issue<true>(c, (SLOT + 3) & 3);
  asm volatile("s_waitcnt vmcnt(%0)" ::"i"(WAITN) : "memory");
  WAIT_L0();
  BAR();
  k2_mma(acc, fb, fa);
  if (LASTBAR) BAR();
}
template <int ISSUE> DI void k2_step4(LAS char* lds, K2Ctx& c, const unsigned (&aA)[2], const unsigned (&aB)[2][4], f32x4 (&acc)[4][4]) {
  k2_step<0, ISSUE, 8, true>(lds, c, aA, aB, acc);
  k2_step<1, ISSUE, 8, true>(lds, c, aA, aB, acc);
  k2_step<2, ISSUE, 8, true>(lds, c, aA, aB, acc);
  k2_step<3, ISSUE, 8, true>(lds, c, aA, aB, acc);
}

__global__ __launch_bounds__(512, 2) void k2_kernel(char* __restrict__ act, const char* __restrict__ qbuf,
                                                    const unsigned short* __restrict__ wphi, const char* __restrict__ wd,
                                                    const char* __restrict__ wo, const float* __restrict__ kvp,
                                                    const float* __restrict__ gate, char* __restrict__ xbuf,
                                                    unsigned* __restrict__ flags, unsigned short* __restrict__ out) {
  extern __shared__ __attribute__((aligned(16))) char smem[];
  LAS char* lds = (LAS char*)smem;
  const unsigned ldsb = (unsigned)(size_t)lds;
  const int tid = threadIdx.x, lane = tid & 63;
  const int wid = __builtin_amdgcn_readfirstlane(tid >> 6);
  const int grp = wid >> 2, j = wid & 3, wm = j >> 1, wn = j & 1;
  const int bid = blockIdx.x, xcd = bid & 7, idx = bid >> 3;
  const int ks = xcd >> 2, qd = xcd & 3;
  const int tm = (qd >> 1) * 8 + (idx & 7), tn = (qd & 1) * 4 + (idx >> 3);
  const int g = lane >> 4, c = lane & 15, q = (lane >> 2) & 3, p = lane & 3;

  STAMP_DECL;
  K2Ctx cx;
  {
    const int r = lane >> 3, ch = lane & 7;
    const int kb = 2 * grp + (j >> 1);
    cx.sA = act + (size_t)(ks * 144 + kb) * PANEL + (tm * 128 + (j & 1) * 64) * 32;
    cx.ldsA = ldsb + kb * 4096 + (j & 1) * 2048;
    cx.vlin = lane * 16;
    const int kr = 32 * grp + 8 * j;
    cx.sB = wd + (size_t)(ks * 2304 + kr) * 2048 + tn * 256;
    cx.voffB = r * 2048 + (((ch >> 1) ^ (((r >> 1) & 1) | ((j & 1) << 1))) * 32) + (ch & 1) * 16;
    cx.ldsB = ldsb + 16384 + kr * 128;
  }
  k2_issue<false>(cx, 0); k2_issue<false>(cx, 1); k2_issue<false>(cx, 2);

  {
    const int head = bid & 31, rb = bid >> 5;
    LAS char* aux = lds + K2_AUX;
    bf16x8 qf[2];
    float gt[2];
#pragma unroll
    for (int m = 0; m < 2; ++m) {
      const int row = rb * 256 + wid * 32 + m * 16 + c;
      qf[m] = *(const bf16x8*)(qbuf + (size_t)(2 * head + (g >> 1)) * PANEL + row * 32 + (g & 1) * 16);
      gt[m] = gate[row];
    }
    for (int e = tid; e < 528; e += 512) {
      f32x4 s = {0.f, 0.f, 0.f, 0.f};
#pragma unroll
      for (int r = 0; r < 8; ++r) s += *(const f32x4*)(kvp + ((size_t)r * 32 + head) * KVP + e * 4);
      *(LAS u32x2*)(aux + e * 8) = pk4(s);
    }
    if (tid < 128) *(LAS u32x4*)(aux + 528 * 8 + tid * 16) = *(const u32x4*)((const char*)wphi + (size_t)head * 2048 + tid * 16);
    __syncthreads();
    bf16x8 wf[2];
#pragma unroll
    for (int fb = 0; fb < 2; ++fb) {
      LAS char* a = aux + 528 * 8 + (8 * g + q) * 64 + fb * 32 + p * 8;
      s16x4 lo = __builtin_amdgcn_ds_read_tr16_b64_v4i16((LAS s16x4*)a);
      s16x4 hi = __builtin_amdgcn_ds_read_tr16_b64_v4i16((LAS s16x4*)(a + 256));
      wf[fb] = __builtin_shufflevector(lo, hi, 0, 1, 2, 3, 4, 5, 6, 7);
    }
    bf16x8 kf[2][2], zf[2];
#pragma unroll
    for (int h = 0; h < 2; ++h) {
#pragma unroll
      for (int db = 0; db < 2; ++db) {
        const u32x2 lo = *(LAS u32x2*)(aux + (((2 * h) * 2 + db) * 64 + lane) * 8);
        const u32x2 hi = *(LAS u32x2*)(aux + (((2 * h + 1) * 2 + db) * 64 + lane) * 8);
        const u32x4 w = {lo[0], lo[1], hi[0], hi[1]};
        kf[h][db] = __builtin_bit_cast(bf16x8, w);
      }
      const u32x2 lo = *(LAS u32x2*)(aux + (512 + (2 * h) * 4 + g) * 8);
      const u32x2 hi = *(LAS u32x2*)(aux + (512 + (2 * h + 1) * 4 + g) * 8);
      const u32x4 w = {lo[0], lo[1], hi[0], hi[1]};
      zf[h] = __builtin_bit_cast(bf16x8, w);
    }
    const f32x4 Z = {0.f, 0.f, 0.f, 0.f};
    f32x4 pp[2][2];
#pragma unroll
    for (int m = 0; m < 2; ++m) { pp[m][0] = MFMA(wf[0], qf[m], Z); pp[m][1] = MFMA(wf[1], qf[m], Z); }
    bf16x8 fp[2], fn[2];
    feature_map2(pp, fp, fn);
    u32x2 w0[2], w1[2];
#pragma unroll
    for (int m = 0; m < 2; ++m) {
      f32x4 n0 = MFMA(kf[0][0], fp[m], Z); n0 = MFMA(kf[1][0], fn[m], n0);
      f32x4 n1 = MFMA(kf[0][1], fp[m], Z); n1 = MFMA(kf[1][1], fn[m], n1);
      f32x4 dn = MFMA(zf[0], fp[m], Z); dn = MFMA(zf[1], fn[m], dn);
      const float sc = gt[m] / (dn[0] + 1e-6f);
      w0[m] = pk4(n0 * sc); w1[m] = pk4(n1 * sc);
    }
    char* op = act + (size_t)(224 + 2 * head) * PANEL + (rb * 256 + wid * 32 + 16 * (g & 1) + c) * 32 + (g >> 1) * 16;
    st16_sc1(op, widen(w0[0], w0[1]));
    st16_sc1(op + PANEL, widen(w1[0], w1[1]));
    WAIT_V(0);
    __syncthreads();
    if (tid == 0) __hip_atomic_fetch_add(flags + (FL_OCNT + rb) * 16, 1u, RLX_AGENT);
  }

  STAMP(1);
  unsigned aA[2], aB[2][4];
  {
    aA[0] = (2 * grp + (g >> 1)) * 4096 + (64 * wm + c) * 32 + (g & 1) * 16;
    aA[1] = aA[0] + 2 * K2_ST;
    const int swz = ((q >> 1) & 1) | ((g & 1) << 1);
#pragma unroll
    for (int i = 0; i < 4; ++i) {
      aB[0][i] = 16384 + wn * 8192 + (32 * grp + 8 * g + q) * 128 + p * 8 + ((i ^ swz) * 32);
      aB[1][i] = aB[0][i] + 2 * K2_ST;
    }
  }
  float og[4];
#pragma unroll
  for (int m = 0; m < 4; ++m) og[m] = 1.0f - gate[tm * 128 + 64 * wm + 16 * m + c];
  asm volatile("" : "+v"(og[0]), "+v"(og[1]), "+v"(og[2]), "+v"(og[3]));
  f32x4 acc[4][4];
#pragma unroll
  for (int i = 0; i < 4; ++i)
#pragma unroll
    for (int m = 0; m < 4; ++m) acc[i][m] = (f32x4){0.f, 0.f, 0.f, 0.f};

  if (grp == 1) BAR();
#pragma unroll 1
  for (int t = 0; t < 16; t += 4) k2_step4<1>(lds, cx, aA, aB, acc);
  k2_step<0, 1, 8, false>(lds, cx, aA, aB, acc);
  STAMP(2);
  if (ks == 1) {
    if (wid == 0) {
      wait_ge(flags + (FL_OCNT + (tm >> 1)) * 16, 32u);
      __builtin_amdgcn_fence(__ATOMIC_ACQUIRE, "agent");
      WAIT_V(0);
    }
    cx.sB = wo + (32 * grp + 8 * j) * 2048 + tn * 256;
  }
  BAR();
  STAMP(3);
  k2_step<1, 2, 8, true>(lds, cx, aA, aB, acc);
  k2_step<2, 2, 8, true>(lds, cx, aA, aB, acc);
  k2_step<3, 2, 8, true>(lds, cx, aA, aB, acc);
  if (ks == 1) {
    MFMA_DRAIN();
#pragma unroll
    for (int i = 0; i < 4; ++i)
#pragma unroll
      for (int m = 0; m < 4; ++m) acc[i][m] *= og[m];
    SCHED(); asm volatile("s_nop 1"); SCHED();
  }
#pragma unroll 1
  for (int t = 20; t < 32; t += 4) k2_step4<2>(lds, cx, aA, aB, acc);
  k2_step<0, 2, 8, true>(lds, cx, aA, aB, acc);
  k2_step<1, 0, 4, true>(lds, cx, aA, aB, acc);
  k2_step<2, 0, 0, true>(lds, cx, aA, aB, acc);
  k2_step<3, 0, 0, false>(lds, cx, aA, aB, acc);
  STAMP(4);
  if (grp == 0) BAR();
  MFMA_DRAIN();
  if (ks == 0) {
#pragma unroll
    for (int i = 0; i < 4; ++i)
#pragma unroll
      for (int m = 0; m < 4; ++m) acc[i][m] *= og[m];
  }

  f32x4 mine[2][4];
  {
    LAS char* wr = lds + wid * 8192 + lane * 16;
    LAS char* rd = lds + (wid ^ 4) * 8192 + lane * 16;
#pragma unroll
    for (int i = 0; i < 2; ++i)
#pragma unroll
      for (int m = 0; m < 4; ++m) {
        *(LAS f32x4*)(wr + (i * 4 + m) * 1024) = sel4(grp, acc[i][m], acc[2 + i][m]);
        mine[i][m] = sel4(grp, acc[2 + i][m], acc[i][m]);
      }
    __syncthreads();
#pragma unroll
    for (int i = 0; i < 2; ++i)
#pragma unroll
      for (int m = 0; m < 4; ++m) mine[i][m] += *(LAS f32x4*)(rd + (i * 4 + m) * 1024);
  }
  STAMP(5);
  {
    char* xs = xbuf + ((size_t)bid * 8 + wid) * 4096 + lane * 16;
#pragma unroll
    for (int i = 0; i < 2; ++i)
#pragma unroll
      for (int m = 0; m < 2; ++m)
        st16_sc1(xs + (i * 2 + m) * 1024, __builtin_bit_cast(u32x4, sel4(ks, mine[i][m], mine[i][2 + m])));
    WAIT_V(0);
    __syncthreads();
    if (tid == 0) __hip_atomic_store(flags + (FL_X + bid) * 16, 1u, RLX_AGENT);
    if (wid == 0) wait_ge(flags + (FL_X + (bid ^ 4)) * 16, 1u);
    __builtin_amdgcn_fence(__ATOMIC_ACQUIRE, "wavefront");
    __syncthreads();
    STAMP(6);
    const char* xr = xbuf + ((size_t)(bid ^ 4) * 8 + wid) * 4096 + lane * 16;
    f32x4 o[4];
    asm volatile("global_load_dwordx4 %0, %4, off sc1\n\tglobal_load_dwordx4 %1, %4, off offset:1024 sc1\n\t"
                 "global_load_dwordx4 %2, %4, off offset:2048 sc1\n\tglobal_load_dwordx4 %3, %4, off offset:3072 sc1\n\t"
                 "s_waitcnt vmcnt(0)"
                 : "=&v"(o[0]), "=&v"(o[1]), "=&v"(o[2]), "=&v"(o[3]) : "v"(xr) : "memory");
#pragma unroll
    for (int m = 0; m < 2; ++m) {
      const f32x4 v0 = sel4(ks, mine[0][2 + m], mine[0][m]) + o[m];
      const f32x4 v1 = sel4(ks, mine[1][2 + m], mine[1][m]) + o[2 + m];
      const u32x4 w = widen(pk4(v0), pk4(v1));
      unsigned short* op = out + (size_t)(tm * 128 + 64 * wm + 32 * ks + 16 * m + c) * D_ + tn * 128 + 64 * wn + 32 * grp + 16 * (g & 1) + 8 * (g >> 1);
      *(u32x4*)op = w;
    }
  }
  STAMP_OUT((char*)gate + (2u << 20), ks);
}

extern "C" void kernel_launch(void* const* d_in, const int* in_sizes, int n_in, void* d_out, int out_size, void* d_ws,
                              size_t ws_size, hipStream_t stream) {
  const unsigned short* x = (const unsigned short*)d_in[0];
  const unsigned short* wqkv = (const unsigned short*)d_in[1];
  const unsigned short* wphi = (const unsigned short*)d_in[2];
  const char* wo = (const char*)d_in[3];
  const unsigned short* wg = (const unsigned short*)d_in[5];
  const unsigned short* wu = (const unsigned short*)d_in[6];
  const char* wd = (const char*)d_in[7];
  const unsigned short* gatew = (const unsigned short*)d_in[8];
  char* ws = (char*)d_ws;
  char* act = ws;
  char* qbuf = ws + (18u << 20);
  float* kvp = (float*)(ws + (22u << 20));
  char* xbuf = ws + (25u << 20) ;
  unsigned* flags = (unsigned*)(ws + (33u << 20));
  float* gate = (float*)(ws + (34u << 20));
  (void)hipFuncSetAttribute((const void*)k1_kernel, hipFuncAttributeMaxDynamicSharedMemorySize, K1_LDS);
  (void)hipFuncSetAttribute((const void*)k2_kernel, hipFuncAttributeMaxDynamicSharedMemorySize, K2_LDS);
  k1_kernel<<<256, 512, K1_LDS, stream>>>(x, wqkv, wphi, wg, wu, gatew, act, qbuf, kvp, gate, flags, wd, wo);
  k2_kernel<<<256, 512, K2_LDS, stream>>>(act, qbuf, wphi, wd, wo, kvp, gate, xbuf, flags, (unsigned short*)d_out);
}
```

```cpp
#include <hip/hip_runtime.h>
#include <hip/hip_bf16.h>
#include <stdint.h>

typedef short bf16x8 __attribute__((ext_vector_type(8)));
typedef short s16x4 __attribute__((ext_vector_type(4)));
typedef float f32x4 __attribute__((ext_vector_type(4)));
typedef float f32x2 __attribute__((ext_vector_type(2)));
typedef unsigned u32x4 __attribute__((ext_vector_type(4)));
typedef unsigned u32x2 __attribute__((ext_vector_type(2)));
typedef __bf16 bf16x2_t __attribute__((ext_vector_type(2)));
#define LAS __attribute__((address_space(3)))
#define GAS __attribute__((address_space(1)))
#define DI __device__ __forceinline__

constexpr int S_ = 2048, D_ = 1024, F_ = 3584;
constexpr int PANEL = S_ * 32;
constexpr int KVP = 2112;

#define STAMP_DECL
#define STAMP(i)
#define STAMP_OUT(base, tag)
DI void glds_s(unsigned voff, const void* sbase, unsigned ldsaddr) {
  asm volatile("s_mov_b32 m0, %2\n\ts_nop 0\n\tglobal_load_lds_dwordx4 %0, %1" ::"v"(voff), "s"(sbase), "s"(ldsaddr) : "memory");
}
DI void glds_v(const void* vaddr, unsigned ldsaddr) {
  asm volatile("s_mov_b32 m0, %1\n\ts_nop 0\n\tglobal_load_lds_dwordx4 %0, off" ::"v"(vaddr), "s"(ldsaddr) : "memory");
}
DI const char* uni_ptr(const char* p) {
  const unsigned long long v = (unsigned long long)p;
  const unsigned lo = __builtin_amdgcn_readfirstlane((unsigned)v), hi = __builtin_amdgcn_readfirstlane((unsigned)(v >> 32));
  return (const char*)(((unsigned long long)hi << 32) | lo);
}
#define WAIT_V(n) asm volatile("s_waitcnt vmcnt(" #n ")" ::: "memory")
#define WAIT_L0() __builtin_amdgcn_s_waitcnt(0xC07F)
#define SCHED() __builtin_amdgcn_sched_barrier(0)
#define BAR() do { SCHED(); asm volatile("" ::: "memory"); __builtin_amdgcn_s_barrier(); asm volatile("" ::: "memory"); SCHED(); } while (0)
#define MFMA_ACC(c, a, b) asm volatile("v_mfma_f32_16x16x32_bf16 %0, %1, %2, %0" : "+v"(c) : "v"(a), "v"(b))
#define MFMA_DRAIN() do { SCHED(); asm volatile("s_nop 7\n\ts_nop 7" ::: "memory"); SCHED(); } while (0)
#define MFMA(a, b, c) __builtin_amdgcn_mfma_f32_16x16x32_bf16((a), (b), (c), 0, 0, 0)

DI unsigned pk2(float lo, float hi) {
  f32x2 v = {lo, hi};
  bf16x2_t r = __builtin_convertvector(v, bf16x2_t);
  return __builtin_bit_cast(unsigned, r);
}
DI bf16x8 pack8(const f32x4& a, const f32x4& b) {
  u32x4 p = {pk2(a[0], a[1]), pk2(a[2], a[3]), pk2(b[0], b[1]), pk2(b[2], b[3])};
  return __builtin_bit_cast(bf16x8, p);
}
DI bf16x8 tr_frag(LAS char* p, int off) {
  s16x4 a = __builtin_amdgcn_ds_read_tr16_b64_v4i16((LAS s16x4*)(p + off));
  s16x4 b = __builtin_amdgcn_ds_read_tr16_b64_v4i16((LAS s16x4*)(p + off + 512));
  return __builtin_shufflevector(a, b, 0, 1, 2, 3, 4, 5, 6, 7);
}
DI float bf2f(unsigned short u) { return __uint_as_float(((unsigned)u) << 16); }
DI unsigned gelu_mul2(f32x2 x, f32x2 u) {
  const float c0 = -2.3022081984f;
  const float c1 = -2.3022081984f * 0.044715f;
  const f32x2 p = __builtin_elementwise_fma(x * x, (f32x2){c1, c1}, (f32x2){c0, c0});
  const f32x2 a = x * p;
  const f32x2 e = {__builtin_amdgcn_exp2f(a[0]), __builtin_amdgcn_exp2f(a[1])};
  const f32x2 d = e + 1.0f;
  const f32x2 r = {__builtin_amdgcn_rcpf(d[0]), __builtin_amdgcn_rcpf(d[1])};
  const f32x2 o = (x * u) * r;
  return pk2(o[0], o[1]);
}
DI void swap16(float& a, float& b) {
  auto r = __builtin_amdgcn_permlane16_swap(__float_as_uint(a), __float_as_uint(b), false, false);
  a = __uint_as_float(r[0]); b = __uint_as_float(r[1]);
}
DI void swap32(float& a, float& b) {
  auto r = __builtin_amdgcn_permlane32_swap(__float_as_uint(a), __float_as_uint(b), false, false);
  a = __uint_as_float(r[0]); b = __uint_as_float(r[1]);
}
template <bool MAX> DI float rop(float a, float b) { return MAX ? fmaxf(a, b) : a + b; }
template <bool MAX> DI void xrow4(float& a, float& b, float& c, float& d) {
  swap32(a, b); float r1 = rop<MAX>(a, b);
  swap32(c, d); float r2 = rop<MAX>(c, d);
  swap16(r1, r2); float r = rop<MAX>(r1, r2);
  float u = r, v = r; swap16(u, v);
  a = u; b = u; swap32(a, b);
  c = v; d = v; swap32(c, d);
}
DI u32x4 widen(u32x2 a, u32x2 b) {
  auto r0 = __builtin_amdgcn_permlane16_swap(a[0], b[0], false, false);
  auto r1 = __builtin_amdgcn_permlane16_swap(a[1], b[1], false, false);
  u32x4 w = {r0[0], r1[0], r0[1], r1[1]};
  return w;
}
DI u32x2 pk4(const f32x4& v) { u32x2 w = {pk2(v[0], v[1]), pk2(v[2], v[3])}; return w; }

DI float max3(float a, float b, float c) { return fmaxf(fmaxf(a, b), c); }
DI float min3(float a, float b, float c) { return fminf(fminf(a, b), c); }
DI f32x4 exp4(const f32x4& p, float s, float o) {
  const f32x4 t = __builtin_elementwise_fma(p, (f32x4){s, s, s, s}, (f32x4){o, o, o, o});
  f32x4 e = {__builtin_amdgcn_exp2f(t[0]), __builtin_amdgcn_exp2f(t[1]), __builtin_amdgcn_exp2f(t[2]), __builtin_amdgcn_exp2f(t[3])};
  return e;
}
DI float sum8(const f32x4& a, const f32x4& b) { const f32x4 t = a + b; const f32x2 u = t.xy + t.zw; return u[0] + u[1]; }
DI void feature_map2(const f32x4 (&p)[2][2], bf16x8 (&fpos)[2], bf16x8 (&fneg)[2]) {
  const float L2E = 1.4426950408889634f;
  float mx[2], nm[2];
#pragma unroll
  for (int t = 0; t < 2; ++t) {
    const f32x4 &p0 = p[t][0], &p1 = p[t][1];
    mx[t] = max3(max3(max3(max3(p0[0], p0[1], p0[2]), p0[3], p1[0]), p1[1], p1[2]), p1[3], p0[0]);
    nm[t] = -min3(min3(min3(min3(p0[0], p0[1], p0[2]), p0[3], p1[0]), p1[1], p1[2]), p1[3], p0[0]);
  }
  xrow4<true>(mx[0], nm[0], mx[1], nm[1]);
  f32x4 e[2][2], n[2][2];
  float sp[2], sn[2];
#pragma unroll
  for (int t = 0; t < 2; ++t) {
    const float a = -mx[t] * L2E, b = -nm[t] * L2E;
#pragma unroll
    for (int h = 0; h < 2; ++h) { e[t][h] = exp4(p[t][h], L2E, a); n[t][h] = exp4(p[t][h], -L2E, b); }
    sp[t] = sum8(e[t][0], e[t][1]);
    sn[t] = sum8(n[t][0], n[t][1]);
  }
  xrow4<false>(sp[0], sn[0], sp[1], sn[1]);
#pragma unroll
  for (int t = 0; t < 2; ++t) {
    const float rp = __builtin_amdgcn_rcpf(sp[t]), rn = __builtin_amdgcn_rcpf(sn[t]);
    fpos[t] = pack8(e[t][0] * rp, e[t][1] * rp);
    fneg[t] = pack8(n[t][0] * rn, n[t][1] * rn);
  }
}

constexpr int K1_A0 = 0, K1_ABUF = 32768, K1_B0 = 65536, K1_BBUF = 40960, K1_WPHI = 147456, K1_DUMP = 147456 + 8192, K1_LDS = 147456 + 8192 + 2048;

constexpr unsigned long long k1_tab(int var, int wn) {
  unsigned long long t = 0;
  for (int i = 0; i < 10; ++i) {
    const int s = i >> 1, half = i & 1;
    int panel = 0, c32 = 0;
    if (var <= 1) {
      const int b = 5 * wn + s;
      if (var == 1) { if (b < 2) { panel = 4; c32 = b + 2 * half; } else { panel = ((b - 2) >> 2) + 2 * half; c32 = (b - 2) & 3; } }
      else          { if (b >= 8) { panel = 4; c32 = b - 8 + 2 * half; } else { panel = (b >> 2) + 2 * half; c32 = b & 3; } }
    } else if (var == 2) {
      if (s < 2) { panel = half; c32 = 2 * wn + s; } else { const int u = 3 * wn + s - 2; panel = 2 + (u >> 1); c32 = 2 * (u & 1) + half; }
    } else if (var == 3) {
      const int u = 5 * wn + s; panel = u >> 1; c32 = 2 * (u & 1) + half;
    } else {
      if (s == 4) { panel = 4; c32 = 2 * wn + half; } else { panel = (s & 1) * 2 + wn; c32 = (s >> 1) * 2 + half; }
    }
    t |= (unsigned long long)(panel * 4 + c32) << (5 * i);
  }
  return t;
}

struct K1Ctx {
  const char* sA;
  const char* sB[4];
  unsigned stepB[4];
  const char* vB4;
  unsigned stepB4;
  unsigned voffA0, voffA1, voffB[4];
  const char* pf;
  unsigned ldsD, vpf;
  unsigned ldsA, ldsB;
};

template <int BUF> DI void k1_issue_A(K1Ctx& c) {
#pragma unroll
  for (int i = 0; i < 4; ++i) glds_s((i & 1) ? c.voffA1 : c.voffA0, c.sA + i * 16384, c.ldsA + BUF * K1_ABUF + i * 1024);
  c.sA += 128;
}
template <int BUF> DI void k1_issue_B(K1Ctx& c) {
#pragma unroll
  for (int i = 0; i < 4; ++i) { glds_s(c.voffB[i], c.sB[i], c.ldsB + BUF * K1_BBUF + i * 8192); c.sB[i] += c.stepB[i]; }
  glds_v(c.vB4, c.ldsB + BUF * K1_BBUF + 4 * 8192); c.vB4 += c.stepB4;
}

template <int BUF, int KS> DI void k1_load(LAS char* lds, const unsigned (&aB)[10], const unsigned (&aA)[2], bf16x8 (&fb)[10], bf16x8 (&fa)[4]) {
#pragma unroll
  for (int m = 0; m < 4; ++m) fa[m] = *(LAS bf16x8*)(lds + aA[KS] + (K1_A0 + BUF * K1_ABUF + m * 2048));
#pragma unroll
  for (int i = 0; i < 10; ++i) fb[i] = tr_frag(lds + aB[i], BUF * K1_BBUF + KS * 4096);
}
DI void k1_mma(f32x4 (&acc)[10][4], const bf16x8 (&fb)[10], const bf16x8 (&fa)[4]) {
  __builtin_amdgcn_s_setprio(1);
#pragma unroll
  for (int i = 0; i < 10; ++i)
#pragma unroll
    for (int m = 0; m < 4; ++m) MFMA_ACC(acc[i][m], fb[i], fa[m]);
  __builtin_amdgcn_s_setprio(0);
}
template <int GRP, int BUF, int MODE>
DI void k1_tile(LAS char* lds, K1Ctx& c, const unsigned (&aB)[10], const unsigned (&aA)[2], f32x4 (&acc)[10][4]) {
  bf16x8 fb[10], fa[4];
  k1_load<BUF, 0>(lds, aB, aA, fb, fa);
  if (MODE < 2) { if (GRP == 0) k1_issue_A<BUF ^ 1>(c); else k1_issue_B<BUF ^ 1>(c); WAIT_V(9); }
  else {
    WAIT_V(0);
    if (c.pf) asm volatile("s_mov_b32 m0, %2\n\ts_nop 0\n\tglobal_load_lds_dword %0, %1" ::"v"(c.vpf), "s"(c.pf), "s"(c.ldsD) : "memory");
  }
  WAIT_L0();
  BAR();
  k1_mma(acc, fb, fa);
  BAR();
  k1_load<BUF, 1>(lds, aB, aA, fb, fa);
  if (MODE == 0) { if (GRP == 0) k1_issue_B<BUF>(c); else k1_issue_A<BUF>(c); WAIT_V(9); }
  else if (MODE == 1) { WAIT_V(4); }
  WAIT_L0();
  BAR();
  k1_mma(acc, fb, fa);
  if (!(MODE == 2 && GRP == 1)) BAR();
}
template <int GRP>
DI void k1_main(LAS char* lds, K1Ctx& c, const unsigned (&aB)[10], const unsigned (&aA)[2], f32x4 (&acc)[10][4]) {
  if (GRP == 1) BAR();
#pragma unroll 1
  for (int t = 0; t < 14; t += 2) {
    k1_tile<GRP, 0, 0>(lds, c, aB, aA, acc);
    k1_tile<GRP, 1, 0>(lds, c, aB, aA, acc);
  }
  k1_tile<GRP, 0, 1>(lds, c, aB, aA, acc);
  k1_tile<GRP, 1, 2>(lds, c, aB, aA, acc);
  MFMA_DRAIN();
}

DI void epi_h(const f32x4 (&ga)[4], const f32x4 (&ua)[4], char* pp) {
#pragma unroll
  for (int mp = 0; mp < 2; ++mp) {
    u32x2 w[2];
#pragma unroll
    for (int h = 0; h < 2; ++h) {
      const int m = 2 * mp + h;
      w[h][0] = gelu_mul2(ga[m].xy, ua[m].xy);
      w[h][1] = gelu_mul2(ga[m].zw, ua[m].zw);
    }
    *(u32x4*)(pp + mp * 1024) = widen(w[0], w[1]);
  }
}
DI void epi_q(const f32x4 (&a0)[4], const f32x4 (&a1)[4], char* pp) {
#pragma unroll
  for (int mp = 0; mp < 2; ++mp) {
    *(u32x4*)(pp + mp * 1024) = widen(pk4(a0[2 * mp]), pk4(a0[2 * mp + 1]));
    *(u32x4*)(pp + PANEL + mp * 1024) = widen(pk4(a1[2 * mp]), pk4(a1[2 * mp + 1]));
  }
}
DI void epi_kv(const bf16x8 (&kp)[4], const bf16x8 (&vp)[4], LAS char* wphi  ,
               LAS char* scr  , LAS char* red  , int lane) {
  const int g = lane >> 4, c = lane & 15, q = (lane >> 2) & 3, p = lane & 3;
  bf16x8 wf[2];
#pragma unroll
  for (int fb = 0; fb < 2; ++fb) {
    LAS char* a = wphi + (4 * g + q) * 64 + fb * 32 + p * 8;
    s16x4 lo = __builtin_amdgcn_ds_read_tr16_b64_v4i16((LAS s16x4*)a);
    s16x4 hi = __builtin_amdgcn_ds_read_tr16_b64_v4i16((LAS s16x4*)(a + 1024));
    wf[fb] = __builtin_shufflevector(lo, hi, 0, 1, 2, 3, 4, 5, 6, 7);
  }
  const u32x4 onesu = {0x3F803F80u, 0x3F803F80u, 0x3F803F80u, 0x3F803F80u};
  const bf16x8 ONES = __builtin_bit_cast(bf16x8, onesu);
  const f32x4 Z = {0.f, 0.f, 0.f, 0.f};
  LAS char* wr = scr + c * 200 + g * 8;
  LAS char* rd = scr + (8 * g + q) * 200 + p * 8;
  f32x4 kv[4][3];
#pragma unroll
  for (int a = 0; a < 4; ++a)
#pragma unroll
    for (int b = 0; b < 3; ++b) kv[a][b] = Z;
#pragma unroll
  for (int mp = 0; mp < 2; ++mp) {
    f32x4 pp[2][2];
#pragma unroll
    for (int h = 0; h < 2; ++h) { pp[h][0] = MFMA(wf[0], kp[2 * mp + h], Z); pp[h][1] = MFMA(wf[1], kp[2 * mp + h], Z); }
    bf16x8 fp[2], fn[2];
    feature_map2(pp, fp, fn);
#pragma unroll
    for (int h = 0; h < 2; ++h) {
      const u32x4 a = __builtin_bit_cast(u32x4, fp[h]), b = __builtin_bit_cast(u32x4, fn[h]), v = __builtin_bit_cast(u32x4, vp[2 * mp + h]);
      LAS char* w = wr + h * 3200;
      *(LAS u32x2*)(w) = a.xy;       *(LAS u32x2*)(w + 32) = a.zw;
      *(LAS u32x2*)(w + 64) = b.xy;  *(LAS u32x2*)(w + 96) = b.zw;
      *(LAS u32x2*)(w + 128) = v.xy; *(LAS u32x2*)(w + 160) = v.zw;
    }
    bf16x8 fr[6];
#pragma unroll
    for (int a = 0; a < 6; ++a) {
      s16x4 lo = __builtin_amdgcn_ds_read_tr16_b64_v4i16((LAS s16x4*)(rd + a * 32));
      s16x4 hi = __builtin_amdgcn_ds_read_tr16_b64_v4i16((LAS s16x4*)(rd + a * 32 + 800));
      fr[a] = __builtin_shufflevector(lo, hi, 0, 1, 2, 3, 4, 5, 6, 7);
    }
#pragma unroll
    for (int a = 0; a < 4; ++a) {
      kv[a][0] = MFMA(fr[a], fr[4], kv[a][0]);
      kv[a][1] = MFMA(fr[a], fr[5], kv[a][1]);
      kv[a][2] = MFMA(fr[a], ONES, kv[a][2]);
    }
  }
#pragma unroll
  for (int a = 0; a < 4; ++a) {
    *(LAS f32x4*)(red + (a * 2 + 0) * 1024 + lane * 16) = kv[a][0];
    *(LAS f32x4*)(red + (a * 2 + 1) * 1024 + lane * 16) = kv[a][1];
    if (c == 0) *(LAS f32x4*)(red + 8192 + a * 64 + g * 16) = kv[a][2];
  }
}

__global__ __launch_bounds__(512, 2) void k1_kernel(const unsigned short* __restrict__ x, const unsigned short* __restrict__ wqkv,
                                                    const unsigned short* __restrict__ wphi, const unsigned short* __restrict__ wg,
                                                    const unsigned short* __restrict__ wu, const unsigned short* __restrict__ gatew,
                                                    char* __restrict__ act, char* __restrict__ qbuf,
                                                    float* __restrict__ kvp, float* __restrict__ gate, unsigned* __restrict__ flags,
                                                    const char* __restrict__ wd, const char* __restrict__ wo) {
  extern __shared__ __attribute__((aligned(16))) char smem[];
  LAS char* lds = (LAS char*)smem;
  const unsigned ldsb = (unsigned)(size_t)lds;
  const int tid = threadIdx.x, lane = tid & 63;
  const int wid = __builtin_amdgcn_readfirstlane(tid >> 6);
  const int grp = wid >> 2, j = wid & 3, wm = 2 * grp + (j >> 1), wn = j & 1;
  const int bid = blockIdx.x, xcd = bid & 7, idx = bid >> 3;
  const int rt = idx & 7, cg = xcd * 4 + (idx >> 3);
  const int row0 = rt * 256;
  const int g = lane >> 4, c = lane & 15;

  STAMP_DECL;
  u32x4 gx0, gx1, gw0, gw1;
  {
    const char* xr = (const char*)x + (size_t)(bid * 8 + wid) * 2048;
    const unsigned vo = lane * 16;
    asm volatile("global_load_dwordx4 %0, %4, %5\n\tglobal_load_dwordx4 %1, %4, %5 offset:1024\n\t"
                 "global_load_dwordx4 %2, %4, %6\n\tglobal_load_dwordx4 %3, %4, %6 offset:1024"
                 : "=&v"(gx0), "=&v"(gx1), "=&v"(gw0), "=&v"(gw1) : "v"(vo), "s"(xr), "s"(gatew) : "memory");
  }

  const int ttype = cg < 22 ? 0 : (cg == 22 ? 1 : (cg == 23 ? 2 : 3));
  const unsigned SW = F_ * 2, SQ = 3 * D_ * 2;
  const char* pb[4]; unsigned ps[4]; const char* n0; const char* n1; unsigned ns;
  {
    const int odd = cg & 1, jj = cg - 24;
    const int wc = cg * 160 + (odd ? 32 : 0), nc = cg * 160 + (odd ? 0 : 128);
    const char* G = (const char*)wg; const char* U = (const char*)wu; const char* Q = (const char*)wqkv;
    const int o0 = ttype == 0 ? wc : (ttype == 1 ? 3520 : (ttype == 2 ? 512 : 1024 + 128 * jj));
    const int o1 = ttype == 0 ? wc + 64 : (ttype == 1 ? 3520 : (ttype == 2 ? 576 : 1088 + 128 * jj));
    const int o2 = ttype == 0 ? wc : (ttype == 1 ? 832 : (ttype == 2 ? 640 : 2048 + 128 * jj));
    const int o3 = ttype == 0 ? wc + 64 : (ttype == 1 ? 896 : (ttype == 2 ? 704 : 2112 + 128 * jj));
    const int o4 = ttype == 0 ? nc : (ttype == 1 ? 960 : (ttype == 2 ? 768 : 64 * jj));
    const int o5 = ttype == 0 ? nc : o4 + 32;
    pb[0] = (ttype <= 1 ? G : Q) + 2 * o0;
    pb[1] = (ttype == 0 ? G : (ttype == 1 ? U : Q)) + 2 * o1;
    pb[2] = (ttype == 0 ? U : Q) + 2 * o2;
    pb[3] = (ttype == 0 ? U : Q) + 2 * o3;
    n0 = (ttype == 0 ? G : Q) + 2 * o4;
    n1 = (ttype == 0 ? U : Q) + 2 * o5;
    ps[0] = ps[1] = ttype <= 1 ? SW : SQ;
    ps[2] = ps[3] = ns = ttype == 0 ? SW : SQ;
  }

  K1Ctx cx;
  {
    const int ar = (grp == 0 ? 128 : 0) + 32 * j;
    cx.sA = uni_ptr((const char*)x + (unsigned)(row0 + ar) * 2048u);
    cx.ldsA = ldsb + K1_A0 + ar * 128;
    const int r = lane >> 3, ch = lane & 7;
    cx.voffA0 = r * 2048 + ((ch ^ (r >> 1)) * 16);
    cx.voffA1 = cx.voffA0 ^ 64;
    const int kr = grp * 32 + 8 * j;
    const int swz = ((r >> 1) & 1) | ((j & 1) << 1);
    const int l32 = (ch >> 1) ^ swz;
    const unsigned coff = l32 * 32 + (ch & 1) * 16;
#pragma unroll
    for (int i = 0; i < 4; ++i) { cx.sB[i] = uni_ptr(pb[i] + (unsigned)kr * ps[i]); cx.stepB[i] = 64 * ps[i]; cx.voffB[i] = r * ps[i] + coff; }
    cx.vB4 = ((l32 & 2) ? n1 : n0) + (size_t)(kr + r) * ns + (coff & 63);
    cx.stepB4 = 64 * ns;
    cx.ldsB = ldsb + K1_B0 + kr * 128;
    const int id = wid * 256 + bid;
    cx.pf = id < 896 ? wd + (size_t)id * 8192 : (id < 1152 ? wo + (size_t)(id - 896) * 8192 : nullptr);
    cx.ldsD = ldsb + K1_DUMP + wid * 256;
    cx.vpf = lane * 128u;
    asm volatile("s_nop 4" : "+s"(cx.sA), "+s"(cx.sB[0]), "+s"(cx.sB[1]), "+s"(cx.sB[2]), "+s"(cx.sB[3]));
  }
  if (ttype == 3) {
    glds_s(lane * 16, (const char*)wphi + (size_t)(cg - 24) * 8192 + wid * 1024, ldsb + K1_WPHI + wid * 1024);
  }
  if (grp == 0) { k1_issue_B<0>(cx); k1_issue_A<0>(cx); k1_issue_B<1>(cx); }
  else          { k1_issue_A<0>(cx); k1_issue_B<0>(cx); k1_issue_A<1>(cx); }

  unsigned aB[10], aA[2];
  {
    const int q = (lane >> 2) & 3, p = lane & 3;
    const int swz = ((q >> 1) & 1) | ((g & 1) << 1);
    const unsigned lb = K1_B0 + (8 * g + q) * 128 + p * 8;
    const int var = ttype == 0 ? (cg & 1) : ttype + 1;
    const unsigned long long t0 = var == 0 ? k1_tab(0, 0) : var == 1 ? k1_tab(1, 0) : var == 2 ? k1_tab(2, 0) : var == 3 ? k1_tab(3, 0) : k1_tab(4, 0);
    const unsigned long long t1 = var == 0 ? k1_tab(0, 1) : var == 1 ? k1_tab(1, 1) : var == 2 ? k1_tab(2, 1) : var == 3 ? k1_tab(3, 1) : k1_tab(4, 1);
    const unsigned long long tb = wn ? t1 : t0;
#pragma unroll
    for (int i = 0; i < 10; ++i) {
      const unsigned code = (unsigned)(tb >> (5 * i)) & 31u;
      aB[i] = lb + (code >> 2) * 8192 + (((code & 3) ^ swz) * 32);
    }
    const int sa = (c >> 1) & 7;
    aA[0] = (64 * wm + c) * 128 + ((g ^ sa) * 16);
    aA[1] = aA[0] ^ 64;
  }

  {
    asm volatile("s_waitcnt vmcnt(13)" : "+v"(gx0), "+v"(gx1), "+v"(gw0), "+v"(gw1)::"memory");
    float s = 0.f;
#pragma unroll
    for (int e = 0; e < 4; ++e) {
      s = __builtin_fmaf(__uint_as_float(gx0[e] << 16), __uint_as_float(gw0[e] << 16), s);
      s = __builtin_fmaf(__uint_as_float(gx0[e] & 0xffff0000u), __uint_as_float(gw0[e] & 0xffff0000u), s);
      s = __builtin_fmaf(__uint_as_float(gx1[e] << 16), __uint_as_float(gw1[e] << 16), s);
      s = __builtin_fmaf(__uint_as_float(gx1[e] & 0xffff0000u), __uint_as_float(gw1[e] & 0xffff0000u), s);
    }
#pragma unroll
    for (int o = 1; o < 64; o <<= 1) s += __shfl_xor(s, o);
    const float gv = 1.0f / (1.0f + __expf(-s));
    if (lane == 0) {
      float* gp = gate + bid * 8 + wid;
      asm volatile("global_store_dword %0, %1, %2" ::"v"(0u), "v"(gv), "s"(gp) : "memory");
    }
  }

  f32x4 acc[10][4];
#pragma unroll
  for (int i = 0; i < 10; ++i)
#pragma unroll
    for (int m = 0; m < 4; ++m) acc[i][m] = (f32x4){0.f, 0.f, 0.f, 0.f};

  WAIT_V(9);
  BAR();
  STAMP(1);
  if (grp == 0) k1_main<0>(lds, cx, aB, aA, acc); else k1_main<1>(lds, cx, aB, aA, acc);

  STAMP(2);
  int tz = threadIdx.x;
  asm volatile("" : "+v"(tz));
  const int lane2 = tz & 63, g2 = lane2 >> 4;
  const int jj = cg - 24;
  if (tz < 8) flags[(16 + bid * 8 + tz) * 16] = 0u;
  if (bid == 0 && tz >= 64 && tz < 72) flags[(tz - 64) * 16] = 0u;
  {
    const unsigned lo = (unsigned)(row0 + 64 * wm + 16 * (g2 & 1) + (lane2 & 15)) * 32u + (g2 >> 1) * 16u;
    char* hp = act + (size_t)(ttype == 0 ? cg * 10 + 5 * wn : 220 + 2 * wn) * PANEL + lo;
    char* qp = qbuf + (size_t)(2 * (ttype == 1 ? 26 + 3 * wn : (ttype == 2 ? 16 + 5 * wn : 2 * jj + wn))) * PANEL + lo;
    const int qs0 = ttype == 1 ? 2 : (ttype == 2 ? 0 : 4);
#pragma unroll
    for (int s = 4; s >= 0; --s) {
      const bool isH = ttype == 0 || (ttype == 1 && s < 2);
      const bool isQ = ttype == 2 || (ttype == 1 && s >= 2) || (ttype == 3 && s == 4);
      if (isH) epi_h(acc[2 * s], acc[2 * s + 1], hp + (size_t)s * PANEL);
      else if (isQ) epi_q(acc[2 * s], acc[2 * s + 1], qp + (size_t)(2 * (s - qs0)) * PANEL);
    }
  }
  STAMP(3);
  if (ttype == 3) {
    LAS char* red = lds + (wm * 4 + 2 * wn) * 8448;
    bf16x8 pk[4][4];
#pragma unroll
    for (int s = 0; s < 4; ++s)
#pragma unroll
      for (int m = 0; m < 4; ++m) { pk[s][m] = pack8(acc[2 * s][m], acc[2 * s + 1][m]); asm volatile("" : "+v"(pk[s][m])); }
    epi_kv(pk[0], pk[1], lds + K1_WPHI + (2 * wn) * 2048, red + 8448, red, lane2);
    epi_kv(pk[2], pk[3], lds + K1_WPHI + (2 * wn + 1) * 2048, red + 8448, red + 8448, lane2);
    STAMP(4);
    __syncthreads();
    STAMP(5);
    for (int e = tz; e < 4 * 528; e += 512) {
      LAS char* p = lds + e * 16;
      f32x4 s = *(LAS f32x4*)p;
      s += *(LAS f32x4*)(p + 4 * 8448);
      s += *(LAS f32x4*)(p + 8 * 8448);
      s += *(LAS f32x4*)(p + 12 * 8448);
      *(f32x4*)(kvp + ((size_t)rt * 32 + 4 * jj) * KVP + e * 4) = s;
    }
  }
  STAMP(6);
  STAMP_OUT((char*)gate + (1u << 20), ttype);
}

constexpr int K2_ST = 32768, K2_NST = 4, K2_AUX = K2_ST * K2_NST, K2_LDS = K2_AUX + 8192;
constexpr int FL_OCNT = 0;
constexpr int FL_X = 16;
#define RLX_AGENT __ATOMIC_RELAXED, __HIP_MEMORY_SCOPE_AGENT

DI void glds_s1(unsigned voff, const void* sbase, unsigned ldsaddr) {
  asm volatile("s_mov_b32 m0, %2\n\ts_nop 0\n\tglobal_load_lds_dwordx4 %0, %1 sc1" ::"v"(voff), "s"(sbase), "s"(ldsaddr) : "memory");
}
DI void st16_sc1(void* p, u32x4 v) { asm volatile("global_store_dwordx4 %0, %1, off sc1\n\ts_nop 1" ::"v"(p), "v"(v) : "memory"); }
DI void wait_ge(const unsigned* p, unsigned need) {
  for (unsigned spins = 0; spins < (1u << 22); ++spins) {
    if ((unsigned)__builtin_amdgcn_readfirstlane(__hip_atomic_load(p, RLX_AGENT)) >= need) break;
    __builtin_amdgcn_s_sleep(1);
  }
}

DI f32x4 sel4(int cnd, f32x4 a, f32x4 b) { return cnd ? a : b; }

struct K2Ctx {
  const char* sA; const char* sB;
  unsigned voffB, vlin;
  unsigned ldsA, ldsB;
};
template <bool SC1> DI void k2_issue(K2Ctx& c, int slot) {
  const unsigned o = slot * K2_ST;
  if (SC1) { glds_s1(c.vlin, c.sA, c.ldsA + o); glds_s1(c.vlin, c.sA + 1024, c.ldsA + o + 1024); }
  else     { glds_s(c.vlin, c.sA, c.ldsA + o); glds_s(c.vlin, c.sA + 1024, c.ldsA + o + 1024); }
  glds_s(c.voffB, c.sB, c.ldsB + o);
  glds_s(c.voffB, c.sB + 128, c.ldsB + o + 8192);
  c.sA += 4 * PANEL;
  c.sB += 64 * 2048;
}
template <int SLOT> DI void k2_load(LAS char* lds, unsigned aA, const unsigned (&aB)[4], bf16x8 (&fb)[4], bf16x8 (&fa)[4]) {
#pragma unroll
  for (int m = 0; m < 4; ++m) fa[m] = *(LAS bf16x8*)(lds + aA + ((SLOT & 1) * K2_ST + m * 512));
#pragma unroll
  for (int i = 0; i < 4; ++i) fb[i] = tr_frag(lds + aB[i], (SLOT & 1) * K2_ST);
}
DI void k2_mma(f32x4 (&acc)[4][4], const bf16x8 (&fb)[4], const bf16x8 (&fa)[4]) {
  __builtin_amdgcn_s_setprio(1);
#pragma unroll
  for (int i = 0; i < 4; ++i)
#pragma unroll
    for (int m = 0; m < 4; ++m) MFMA_ACC(acc[i][m], fb[i], fa[m]);
  __builtin_amdgcn_s_setprio(0);
}
template <int SLOT, int ISSUE, int WAITN, bool LASTBAR>
DI void k2_step(LAS char* lds, K2Ctx& c, const unsigned (&aA)[2], const unsigned (&aB)[2][4], f32x4 (&acc)[4][4]) {
  bf16x8 fb[4], fa[4];
  k2_load<SLOT>(lds, aA[SLOT >> 1], aB[SLOT >> 1], fb, fa);
  if (ISSUE == 1) k2_issue<false>(c, (SLOT + 3) & 3);
  if (ISSUE == 2) k2_issue<true>(c, (SLOT + 3) & 3);
  asm volatile("s_waitcnt vmcnt(%0)" ::"i"(WAITN) : "memory");
  WAIT_L0();
  BAR();
  k2_mma(acc, fb, fa);
  if (LASTBAR) BAR();
}
template <int ISSUE> DI void k2_step4(LAS char* lds, K2Ctx& c, const unsigned (&aA)[2], const unsigned (&aB)[2][4], f32x4 (&acc)[4][4]) {
  k2_step<0, ISSUE, 8, true>(lds, c, aA, aB, acc);
  k2_step<1, ISSUE, 8, true>(lds, c, aA, aB, acc);
  k2_step<2, ISSUE, 8, true>(lds, c, aA, aB, acc);
  k2_step<3, ISSUE, 8, true>(lds, c, aA, aB, acc);
}

__global__ __launch_bounds__(512, 2) void k2_kernel(char* __restrict__ act, const char* __restrict__ qbuf,
                                                    const unsigned short* __restrict__ wphi, const char* __restrict__ wd,
                                                    const char* __restrict__ wo, const float* __restrict__ kvp,
                                                    const float* __restrict__ gate, char* __restrict__ xbuf,
                                                    unsigned* __restrict__ flags, unsigned short* __restrict__ out) {
  extern __shared__ __attribute__((aligned(16))) char smem[];
  LAS char* lds = (LAS char*)smem;
  const unsigned ldsb = (unsigned)(size_t)lds;
  const int tid = threadIdx.x, lane = tid & 63;
  const int wid = __builtin_amdgcn_readfirstlane(tid >> 6);
  const int grp = wid >> 2, j = wid & 3, wm = j >> 1, wn = j & 1;
  const int bid = blockIdx.x, xcd = bid & 7, idx = bid >> 3;
  const int ks = xcd >> 2, qd = xcd & 3;
  const int tm = (qd >> 1) * 8 + (idx & 7), tn = (qd & 1) * 4 + (idx >> 3);
  const int g = lane >> 4, c = lane & 15, q = (lane >> 2) & 3, p = lane & 3;

  STAMP_DECL;
  K2Ctx cx;
  {
    const int r = lane >> 3, ch = lane & 7;
    const int kb = 2 * grp + (j >> 1);
    cx.sA = act + (size_t)(ks * 144 + kb) * PANEL + (tm * 128 + (j & 1) * 64) * 32;
    cx.ldsA = ldsb + kb * 4096 + (j & 1) * 2048;
    cx.vlin = lane * 16;
    const int kr = 32 * grp + 8 * j;
    cx.sB = wd + (size_t)(ks * 2304 + kr) * 2048 + tn * 256;
    cx.voffB = r * 2048 + (((ch >> 1) ^ (((r >> 1) & 1) | ((j & 1) << 1))) * 32) + (ch & 1) * 16;
    cx.ldsB = ldsb + 16384 + kr * 128;
  }
  const int head = bid & 31, rb = bid >> 5;
  float og[4];
  {
    LAS char* aux = lds + K2_AUX;
    const char* kvb = (const char*)kvp + (size_t)head * (KVP * 4);
    constexpr unsigned RS = 32u * KVP * 4u;
    f32x4 zq[8], kq[8];
    if (wid == 0) {
      const unsigned vo = (512 + c) * 16;
      asm volatile("global_load_dwordx4 %0, %8, %9\n\tglobal_load_dwordx4 %1, %8, %10\n\tglobal_load_dwordx4 %2, %8, %11\n\tglobal_load_dwordx4 %3, %8, %12\n\t"
                   "global_load_dwordx4 %4, %8, %13\n\tglobal_load_dwordx4 %5, %8, %14\n\tglobal_load_dwordx4 %6, %8, %15\n\tglobal_load_dwordx4 %7, %8, %16"
                   : "=&v"(zq[0]), "=&v"(zq[1]), "=&v"(zq[2]), "=&v"(zq[3]), "=&v"(zq[4]), "=&v"(zq[5]), "=&v"(zq[6]), "=&v"(zq[7])
                   : "v"(vo), "s"(kvb), "s"(kvb + RS), "s"(kvb + 2 * RS), "s"(kvb + 3 * RS), "s"(kvb + 4 * RS), "s"(kvb + 5 * RS), "s"(kvb + 6 * RS), "s"(kvb + 7 * RS)
                   : "memory");
    } else if (wid <= 2) {
      glds_s(lane * 16, (const char*)wphi + (size_t)head * 2048 + (wid - 1) * 1024, ldsb + K2_AUX + 528 * 8 + (wid - 1) * 1024);
    }
    {
      const unsigned vo = tid * 16;
      asm volatile("global_load_dwordx4 %0, %8, %9\n\tglobal_load_dwordx4 %1, %8, %10\n\tglobal_load_dwordx4 %2, %8, %11\n\tglobal_load_dwordx4 %3, %8, %12\n\t"
                   "global_load_dwordx4 %4, %8, %13\n\tglobal_load_dwordx4 %5, %8, %14\n\tglobal_load_dwordx4 %6, %8, %15\n\tglobal_load_dwordx4 %7, %8, %16"
                   : "=&v"(kq[0]), "=&v"(kq[1]), "=&v"(kq[2]), "=&v"(kq[3]), "=&v"(kq[4]), "=&v"(kq[5]), "=&v"(kq[6]), "=&v"(kq[7])
                   : "v"(vo), "s"(kvb), "s"(kvb + RS), "s"(kvb + 2 * RS), "s"(kvb + 3 * RS), "s"(kvb + 4 * RS), "s"(kvb + 5 * RS), "s"(kvb + 6 * RS), "s"(kvb + 7 * RS)
                   : "memory");
    }
    bf16x8 qf[2];
    float gt[2];
    {
      const char* qb = qbuf + (size_t)(2 * head) * PANEL + (rb * 256 + wid * 32) * 32;
      const unsigned vq = (g >> 1) * PANEL + c * 32 + (g & 1) * 16, vg = c * 4;
      const float* g0 = gate + rb * 256 + wid * 32;
      const float* g1 = gate + tm * 128 + 64 * wm;
      asm volatile("global_load_dwordx4 %0, %8, %10\n\tglobal_load_dwordx4 %1, %8, %10 offset:512\n\t"
                   "global_load_dword %2, %9, %11\n\tglobal_load_dword %3, %9, %11 offset:64\n\t"
                   "global_load_dword %4, %9, %12\n\tglobal_load_dword %5, %9, %12 offset:64\n\t"
                   "global_load_dword %6, %9, %12 offset:128\n\tglobal_load_dword %7, %9, %12 offset:192"
                   : "=&v"(qf[0]), "=&v"(qf[1]), "=&v"(gt[0]), "=&v"(gt[1]), "=&v"(og[0]), "=&v"(og[1]), "=&v"(og[2]), "=&v"(og[3])
                   : "v"(vq), "v"(vg), "s"(qb), "s"(g0), "s"(g1) : "memory");
    }
    k2_issue<false>(cx, 0); k2_issue<false>(cx, 1); k2_issue<false>(cx, 2);
    asm volatile("s_waitcnt vmcnt(20)" : "+v"(kq[0]), "+v"(kq[1]), "+v"(kq[2]), "+v"(kq[3]), "+v"(kq[4]), "+v"(kq[5]), "+v"(kq[6]), "+v"(kq[7])::"memory");
    *(LAS u32x2*)(aux + tid * 8) = pk4(((kq[0] + kq[1]) + (kq[2] + kq[3])) + ((kq[4] + kq[5]) + (kq[6] + kq[7])));
    if (wid == 0) {
      asm volatile("" : "+v"(zq[0]), "+v"(zq[1]), "+v"(zq[2]), "+v"(zq[3]), "+v"(zq[4]), "+v"(zq[5]), "+v"(zq[6]), "+v"(zq[7]));
      *(LAS u32x2*)(aux + (512 + c) * 8) = pk4(((zq[0] + zq[1]) + (zq[2] + zq[3])) + ((zq[4] + zq[5]) + (zq[6] + zq[7])));
    }
    asm volatile("s_waitcnt vmcnt(12)" : "+v"(qf[0]), "+v"(qf[1]), "+v"(gt[0]), "+v"(gt[1]), "+v"(og[0]), "+v"(og[1]), "+v"(og[2]), "+v"(og[3])::"memory");
    WAIT_L0();
    BAR();
    bf16x8 wf[2];
#pragma unroll
    for (int fb = 0; fb < 2; ++fb) {
      LAS char* a = aux + 528 * 8 + (8 * g + q) * 64 + fb * 32 + p * 8;
      s16x4 lo = __builtin_amdgcn_ds_read_tr16_b64_v4i16((LAS s16x4*)a);
      s16x4 hi = __builtin_amdgcn_ds_read_tr16_b64_v4i16((LAS s16x4*)(a + 256));
      wf[fb] = __builtin_shufflevector(lo, hi, 0, 1, 2, 3, 4, 5, 6, 7);
    }
    bf16x8 kf[2][2], zf[2];
#pragma unroll
    for (int h = 0; h < 2; ++h) {
#pragma unroll
      for (int db = 0; db < 2; ++db) {
        const u32x2 lo = *(LAS u32x2*)(aux + (((2 * h) * 2 + db) * 64 + lane) * 8);
        const u32x2 hi = *(LAS u32x2*)(aux + (((2 * h + 1) * 2 + db) * 64 + lane) * 8);
        const u32x4 w = {lo[0], lo[1], hi[0], hi[1]};
        kf[h][db] = __builtin_bit_cast(bf16x8, w);
      }
      const u32x2 lo = *(LAS u32x2*)(aux + (512 + (2 * h) * 4 + g) * 8);
      const u32x2 hi = *(LAS u32x2*)(aux + (512 + (2 * h + 1) * 4 + g) * 8);
      const u32x4 w = {lo[0], lo[1], hi[0], hi[1]};
      zf[h] = __builtin_bit_cast(bf16x8, w);
    }
    const f32x4 Z = {0.f, 0.f, 0.f, 0.f};
    f32x4 pp[2][2];
#pragma unroll
    for (int m = 0; m < 2; ++m) { pp[m][0] = MFMA(wf[0], qf[m], Z); pp[m][1] = MFMA(wf[1], qf[m], Z); }
    bf16x8 fp[2], fn[2];
    feature_map2(pp, fp, fn);
    u32x2 w0[2], w1[2];
#pragma unroll
    for (int m = 0; m < 2; ++m) {
      f32x4 n0 = MFMA(kf[0][0], fp[m], Z); n0 = MFMA(kf[1][0], fn[m], n0);
      f32x4 n1 = MFMA(kf[0][1], fp[m], Z); n1 = MFMA(kf[1][1], fn[m], n1);
      f32x4 dn = MFMA(zf[0], fp[m], Z); dn = MFMA(zf[1], fn[m], dn);
      const float sc = gt[m] / (dn[0] + 1e-6f);
      w0[m] = pk4(n0 * sc); w1[m] = pk4(n1 * sc);
    }
    char* op = act + (size_t)(224 + 2 * head) * PANEL + (rb * 256 + wid * 32 + 16 * (g & 1) + c) * 32 + (g >> 1) * 16;
    st16_sc1(op, widen(w0[0], w0[1]));
    st16_sc1(op + PANEL, widen(w1[0], w1[1]));
  }

  STAMP(1);
  unsigned aA[2], aB[2][4];
  {
    aA[0] = (2 * grp + (g >> 1)) * 4096 + (64 * wm + c) * 32 + (g & 1) * 16;
    aA[1] = aA[0] + 2 * K2_ST;
    const int swz = ((q >> 1) & 1) | ((g & 1) << 1);
#pragma unroll
    for (int i = 0; i < 4; ++i) {
      aB[0][i] = 16384 + wn * 8192 + (32 * grp + 8 * g + q) * 128 + p * 8 + ((i ^ swz) * 32);
      aB[1][i] = aB[0][i] + 2 * K2_ST;
    }
  }
#pragma unroll
  for (int m = 0; m < 4; ++m) og[m] = 1.0f - og[m];
  f32x4 acc[4][4];
#pragma unroll
  for (int i = 0; i < 4; ++i)
#pragma unroll
    for (int m = 0; m < 4; ++m) acc[i][m] = (f32x4){0.f, 0.f, 0.f, 0.f};

  WAIT_V(10);
  BAR();
  if (grp == 1) BAR();
#pragma unroll 1
  for (int t = 0; t < 16; t += 4) {
    if (t == 4 && tid == 0) __hip_atomic_fetch_add(flags + (FL_OCNT + rb) * 16, 1u, RLX_AGENT);
    k2_step4<1>(lds, cx, aA, aB, acc);
  }
  k2_step<0, 1, 8, false>(lds, cx, aA, aB, acc);
  STAMP(2);
  if (ks == 1) {
    if (wid == 0) {
      wait_ge(flags + (FL_OCNT + (tm >> 1)) * 16, 32u);
    }
    cx.sB = wo + (32 * grp + 8 * j) * 2048 + tn * 256;
  }
  BAR();
  STAMP(3);
  k2_step<1, 2, 8, true>(lds, cx, aA, aB, acc);
  k2_step<2, 2, 8, true>(lds, cx, aA, aB, acc);
  k2_step<3, 2, 8, true>(lds, cx, aA, aB, acc);
  if (ks == 1) {
    MFMA_DRAIN();
#pragma unroll
    for (int i = 0; i < 4; ++i)
#pragma unroll
      for (int m = 0; m < 4; ++m) acc[i][m] *= og[m];
    SCHED(); asm volatile("s_nop 1"); SCHED();
  }
#pragma unroll 1
  for (int t = 20; t < 32; t += 4) k2_step4<2>(lds, cx, aA, aB, acc);
  k2_step<0, 2, 8, true>(lds, cx, aA, aB, acc);
  k2_step<1, 0, 4, true>(lds, cx, aA, aB, acc);
  k2_step<2, 0, 0, true>(lds, cx, aA, aB, acc);
  k2_step<3, 0, 0, false>(lds, cx, aA, aB, acc);
  STAMP(4);
  if (grp == 0) BAR();
  MFMA_DRAIN();
  if (ks == 0) {
#pragma unroll
    for (int i = 0; i < 4; ++i)
#pragma unroll
      for (int m = 0; m < 4; ++m) acc[i][m] *= og[m];
  }

  f32x4 mine[2][4];
  {
    LAS char* wr = lds + wid * 8192 + lane * 16;
    LAS char* rd = lds + (wid ^ 4) * 8192 + lane * 16;
#pragma unroll
    for (int i = 0; i < 2; ++i)
#pragma unroll
      for (int m = 0; m < 4; ++m) {
        *(LAS f32x4*)(wr + (i * 4 + m) * 1024) = sel4(grp, acc[i][m], acc[2 + i][m]);
        mine[i][m] = sel4(grp, acc[2 + i][m], acc[i][m]);
      }
    __syncthreads();
#pragma unroll
    for (int i = 0; i < 2; ++i)
#pragma unroll
      for (int m = 0; m < 4; ++m) mine[i][m] += *(LAS f32x4*)(rd + (i * 4 + m) * 1024);
  }
  STAMP(5);
  {
    char* xs = xbuf + ((size_t)bid * 8 + wid) * 4096 + lane * 16;
#pragma unroll
    for (int i = 0; i < 2; ++i)
#pragma unroll
      for (int m = 0; m < 2; ++m)
        st16_sc1(xs + (i * 2 + m) * 1024, __builtin_bit_cast(u32x4, sel4(ks, mine[i][m], mine[i][2 + m])));
    WAIT_V(0);
    if (lane == 0) __hip_atomic_store(flags + (FL_X + bid * 8 + wid) * 16, 1u, RLX_AGENT);
    wait_ge(flags + (FL_X + (bid ^ 4) * 8 + wid) * 16, 1u);
    __builtin_amdgcn_fence(__ATOMIC_ACQUIRE, "wavefront");
    STAMP(6);
    const char* xr = xbuf + ((size_t)(bid ^ 4) * 8 + wid) * 4096 + lane * 16;
    f32x4 o[4];
    asm volatile("global_load_dwordx4 %0, %4, off sc1\n\tglobal_load_dwordx4 %1, %4, off offset:1024 sc1\n\t"
                 "global_load_dwordx4 %2, %4, off offset:2048 sc1\n\tglobal_load_dwordx4 %3, %4, off offset:3072 sc1\n\t"
                 "s_waitcnt vmcnt(0)"
                 : "=&v"(o[0]), "=&v"(o[1]), "=&v"(o[2]), "=&v"(o[3]) : "v"(xr) : "memory");
#pragma unroll
    for (int m = 0; m < 2; ++m) {
      const f32x4 v0 = sel4(ks, mine[0][2 + m], mine[0][m]) + o[m];
      const f32x4 v1 = sel4(ks, mine[1][2 + m], mine[1][m]) + o[2 + m];
      const u32x4 w = widen(pk4(v0), pk4(v1));
      unsigned short* op = out + (size_t)(tm * 128 + 64 * wm + 32 * ks + 16 * m + c) * D_ + tn * 128 + 64 * wn + 32 * grp + 16 * (g & 1) + 8 * (g >> 1);
      *(u32x4*)op = w;
    }
  }
  STAMP_OUT((char*)gate + (2u << 20), ks);
}

extern "C" void kernel_launch(void* const* d_in, const int* in_sizes, int n_in, void* d_out, int out_size, void* d_ws,
                              size_t ws_size, hipStream_t stream) {
  const unsigned short* x = (const unsigned short*)d_in[0];
  const unsigned short* wqkv = (const unsigned short*)d_in[1];
  const unsigned short* wphi = (const unsigned short*)d_in[2];
  const char* wo = (const char*)d_in[3];
  const unsigned short* wg = (const unsigned short*)d_in[5];
  const unsigned short* wu = (const unsigned short*)d_in[6];
  const char* wd = (const char*)d_in[7];
  const unsigned short* gatew = (const unsigned short*)d_in[8];
  char* ws = (char*)d_ws;
  char* act = ws;
  char* qbuf = ws + (18u << 20);
  float* kvp = (float*)(ws + (22u << 20));
  char* xbuf = ws + (25u << 20) ;
  unsigned* flags = (unsigned*)(ws + (33u << 20));
  float* gate = (float*)(ws + (34u << 20));
  (void)hipFuncSetAttribute((const void*)k1_kernel, hipFuncAttributeMaxDynamicSharedMemorySize, K1_LDS);
  (void)hipFuncSetAttribute((const void*)k2_kernel, hipFuncAttributeMaxDynamicSharedMemorySize, K2_LDS);
  k1_kernel<<<256, 512, K1_LDS, stream>>>(x, wqkv, wphi, wg, wu, gatew, act, qbuf, kvp, gate, flags, wd, wo);
  k2_kernel<<<256, 512, K2_LDS, stream>>>(act, qbuf, wphi, wd, wo, kvp, gate, xbuf, flags, (unsigned short*)d_out);
}
```

```cpp
#include <hip/hip_runtime.h>
#include <hip/hip_bf16.h>
#include <stdint.h>

typedef short bf16x8 __attribute__((ext_vector_type(8)));
typedef short s16x4 __attribute__((ext_vector_type(4)));
typedef float f32x4 __attribute__((ext_vector_type(4)));
typedef float f32x2 __attribute__((ext_vector_type(2)));
typedef unsigned u32x4 __attribute__((ext_vector_type(4)));
typedef unsigned u32x2 __attribute__((ext_vector_type(2)));
typedef __bf16 bf16x2_t __attribute__((ext_vector_type(2)));
#define LAS __attribute__((address_space(3)))
#define GAS __attribute__((address_space(1)))
#define DI __device__ __forceinline__

constexpr int S_ = 2048, D_ = 1024, F_ = 3584;
constexpr int PANEL = S_ * 32;
constexpr int KVP = 2112;

#define STAMP_DECL
#define STAMP(i)
#define STAMP_OUT(base, tag)
DI void glds_s(unsigned voff, const void* sbase, unsigned ldsaddr) {
  asm volatile("s_mov_b32 m0, %2\n\ts_nop 0\n\tglobal_load_lds_dwordx4 %0, %1" ::"v"(voff), "s"(sbase), "s"(ldsaddr) : "memory");
}
DI void glds_v(const void* vaddr, unsigned ldsaddr) {
  asm volatile("s_mov_b32 m0, %1\n\ts_nop 0\n\tglobal_load_lds_dwordx4 %0, off" ::"v"(vaddr), "s"(ldsaddr) : "memory");
}
DI const char* uni_ptr(const char* p) {
  const unsigned long long v = (unsigned long long)p;
  const unsigned lo = __builtin_amdgcn_readfirstlane((unsigned)v), hi = __builtin_amdgcn_readfirstlane((unsigned)(v >> 32));
  return (const char*)(((unsigned long long)hi << 32) | lo);
}
#define WAIT_V(n) asm volatile("s_waitcnt vmcnt(" #n ")" ::: "memory")
#define WAIT_L0() __builtin_amdgcn_s_waitcnt(0xC07F)
#define SCHED() __builtin_amdgcn_sched_barrier(0)
#define BAR() do { SCHED(); asm volatile("" ::: "memory"); __builtin_amdgcn_s_barrier(); asm volatile("" ::: "memory"); SCHED(); } while (0)
#define MFMA_ACC(c, a, b) asm volatile("v_mfma_f32_16x16x32_bf16 %0, %1, %2, %0" : "+v"(c) : "v"(a), "v"(b))
#define MFMA_DRAIN() do { SCHED(); asm volatile("s_nop 7\n\ts_nop 7" ::: "memory"); SCHED(); } while (0)
#define MFMA(a, b, c) __builtin_amdgcn_mfma_f32_16x16x32_bf16((a), (b), (c), 0, 0, 0)

DI unsigned pk2(float lo, float hi) {
  f32x2 v = {lo, hi};
  bf16x2_t r = __builtin_convertvector(v, bf16x2_t);
  return __builtin_bit_cast(unsigned, r);
}
DI bf16x8 pack8(const f32x4& a, const f32x4& b) {
  u32x4 p = {pk2(a[0], a[1]), pk2(a[2], a[3]), pk2(b[0], b[1]), pk2(b[2], b[3])};
  return __builtin_bit_cast(bf16x8, p);
}
DI bf16x8 tr_frag(LAS char* p, int off) {
  s16x4 a = __builtin_amdgcn_ds_read_tr16_b64_v4i16((LAS s16x4*)(p + off));
  s16x4 b = __builtin_amdgcn_ds_read_tr16_b64_v4i16((LAS s16x4*)(p + off + 512));
  return __builtin_shufflevector(a, b, 0, 1, 2, 3, 4, 5, 6, 7);
}
DI float bf2f(unsigned short u) { return __uint_as_float(((unsigned)u) << 16); }
DI unsigned gelu_mul2(f32x2 x, f32x2 u) {
  const float c0 = -2.3022081984f;
  const float c1 = -2.3022081984f * 0.044715f;
  const f32x2 p = __builtin_elementwise_fma(x * x, (f32x2){c1, c1}, (f32x2){c0, c0});
  const f32x2 a = x * p;
  const f32x2 e = {__builtin_amdgcn_exp2f(a[0]), __builtin_amdgcn_exp2f(a[1])};
  const f32x2 d = e + 1.0f;
  const f32x2 r = {__builtin_amdgcn_rcpf(d[0]), __builtin_amdgcn_rcpf(d[1])};
  const f32x2 o = (x * u) * r;
  return pk2(o[0], o[1]);
}
DI void swap16(float& a, float& b) {
  auto r = __builtin_amdgcn_permlane16_swap(__float_as_uint(a), __float_as_uint(b), false, false);
  a = __uint_as_float(r[0]); b = __uint_as_float(r[1]);
}
DI void swap32(float& a, float& b) {
  auto r = __builtin_amdgcn_permlane32_swap(__float_as_uint(a), __float_as_uint(b), false, false);
  a = __uint_as_float(r[0]); b = __uint_as_float(r[1]);
}
template <bool MAX> DI float rop(float a, float b) { return MAX ? fmaxf(a, b) : a + b; }
template <bool MAX> DI void xrow4(float& a, float& b, float& c, float& d) {
  swap32(a, b); float r1 = rop<MAX>(a, b);
  swap32(c, d); float r2 = rop<MAX>(c, d);
  swap16(r1, r2); float r = rop<MAX>(r1, r2);
  float u = r, v = r; swap16(u, v);
  a = u; b = u; swap32(a, b);
  c = v; d = v; swap32(c, d);
}
DI u32x4 widen(u32x2 a, u32x2 b) {
  auto r0 = __builtin_amdgcn_permlane16_swap(a[0], b[0], false, false);
  auto r1 = __builtin_amdgcn_permlane16_swap(a[1], b[1], false, false);
  u32x4 w = {r0[0], r1[0], r0[1], r1[1]};
  return w;
}
DI u32x2 pk4(const f32x4& v) { u32x2 w = {pk2(v[0], v[1]), pk2(v[2], v[3])}; return w; }

DI float max3(float a, float b, float c) { return fmaxf(fmaxf(a, b), c); }
DI float min3(float a, float b, float c) { return fminf(fminf(a, b), c); }
DI f32x4 exp4(const f32x4& p, float s, float o) {
  const f32x4 t = __builtin_elementwise_fma(p, (f32x4){s, s, s, s}, (f32x4){o, o, o, o});
  f32x4 e = {__builtin_amdgcn_exp2f(t[0]), __builtin_amdgcn_exp2f(t[1]), __builtin_amdgcn_exp2f(t[2]), __builtin_amdgcn_exp2f(t[3])};
  return e;
}
DI float sum8(const f32x4& a, const f32x4& b) { const f32x4 t = a + b; const f32x2 u = t.xy + t.zw; return u[0] + u[1]; }
DI void feature_map2(const f32x4 (&p)[2][2], bf16x8 (&fpos)[2], bf16x8 (&fneg)[2]) {
  const float L2E = 1.4426950408889634f;
  float mx[2], nm[2];
#pragma unroll
  for (int t = 0; t < 2; ++t) {
    const f32x4 &p0 = p[t][0], &p1 = p[t][1];
    mx[t] = max3(max3(max3(max3(p0[0], p0[1], p0[2]), p0[3], p1[0]), p1[1], p1[2]), p1[3], p0[0]);
    nm[t] = -min3(min3(min3(min3(p0[0], p0[1], p0[2]), p0[3], p1[0]), p1[1], p1[2]), p1[3], p0[0]);
  }
  xrow4<true>(mx[0], nm[0], mx[1], nm[1]);
  f32x4 e[2][2], n[2][2];
  float sp[2], sn[2];
#pragma unroll
  for (int t = 0; t < 2; ++t) {
    const float a = -mx[t] * L2E, b = -nm[t] * L2E;
#pragma unroll
    for (int h = 0; h < 2; ++h) { e[t][h] = exp4(p[t][h], L2E, a); n[t][h] = exp4(p[t][h], -L2E, b); }
    sp[t] = sum8(e[t][0], e[t][1]);
    sn[t] = sum8(n[t][0], n[t][1]);
  }
  xrow4<false>(sp[0], sn[0], sp[1], sn[1]);
#pragma unroll
  for (int t = 0; t < 2; ++t) {
    const float rp = __builtin_amdgcn_rcpf(sp[t]), rn = __builtin_amdgcn_rcpf(sn[t]);
    fpos[t] = pack8(e[t][0] * rp, e[t][1] * rp);
    fneg[t] = pack8(n[t][0] * rn, n[t][1] * rn);
  }
}

constexpr int K1_A0 = 0, K1_ABUF = 32768, K1_B0 = 65536, K1_BBUF = 40960, K1_WPHI = 147456, K1_DUMP = 147456 + 2048, K1_LDS = 147456 + 2048 + 2048;

constexpr unsigned long long k1_tab(int wn) {
  const int c0[10] = {0, 4, 1, 5, 2, 6, 3, 7, 8, 11};
  const int c1[10] = {9, 12, 10, 13, 14, 15, 16, 17, 18, 19};
  unsigned long long t = 0;
  for (int i = 0; i < 10; ++i) t |= (unsigned long long)(wn ? c1[i] : c0[i]) << (5 * i);
  return t;
}
constexpr unsigned SW = F_ * 2, SQ = 3 * D_ * 2;

struct K1Ctx {
  const char* sA;
  const char* sG; const char* sU; const char* sQ;
  const char* vP2; const char* vP3;
  unsigned stepP3;
  unsigned voffA0, voffA1, voffW, voffQ;
  const char* pf;
  unsigned ldsD, vpf;
  unsigned ldsA, ldsB;
};

template <int BUF> DI void k1_issue_A(K1Ctx& c) {
#pragma unroll
  for (int i = 0; i < 4; ++i) glds_s((i & 1) ? c.voffA1 : c.voffA0, c.sA + i * 16384, c.ldsA + BUF * K1_ABUF + i * 1024);
  c.sA += 128;
}
template <int BUF> DI void k1_issue_B(K1Ctx& c) {
  const unsigned l = c.ldsB + BUF * K1_BBUF;
  glds_s(c.voffW, c.sG, l);             c.sG += 64 * SW;
  glds_s(c.voffW, c.sU, l + 8192);      c.sU += 64 * SW;
  glds_v(c.vP2, l + 2 * 8192);          c.vP2 += 64 * SW;
  glds_v(c.vP3, l + 3 * 8192);          c.vP3 += c.stepP3;
  glds_s(c.voffQ, c.sQ, l + 4 * 8192);  c.sQ += 64 * SQ;
}

template <int BUF, int KS> DI void k1_load(LAS char* lds, const unsigned (&aB)[10], const unsigned (&aA)[2], bf16x8 (&fb)[10], bf16x8 (&fa)[4]) {
#pragma unroll
  for (int m = 0; m < 4; ++m) fa[m] = *(LAS bf16x8*)(lds + aA[KS] + (K1_A0 + BUF * K1_ABUF + m * 2048));
#pragma unroll
  for (int i = 0; i < 10; ++i) fb[i] = tr_frag(lds + aB[i], BUF * K1_BBUF + KS * 4096);
}
DI void k1_mma(f32x4 (&acc)[10][4], const bf16x8 (&fb)[10], const bf16x8 (&fa)[4]) {
  __builtin_amdgcn_s_setprio(1);
#pragma unroll
  for (int i = 0; i < 10; ++i)
#pragma unroll
    for (int m = 0; m < 4; ++m) MFMA_ACC(acc[i][m], fb[i], fa[m]);
  __builtin_amdgcn_s_setprio(0);
}
template <int GRP, int BUF, int MODE>
DI void k1_tile(LAS char* lds, K1Ctx& c, const unsigned (&aB)[10], const unsigned (&aA)[2], f32x4 (&acc)[10][4]) {
  bf16x8 fb[10], fa[4];
  k1_load<BUF, 0>(lds, aB, aA, fb, fa);
  if (MODE < 2) { if (GRP == 0) k1_issue_A<BUF ^ 1>(c); else k1_issue_B<BUF ^ 1>(c); WAIT_V(9); }
  else {
    WAIT_V(0);
    if (c.pf) asm volatile("s_mov_b32 m0, %2\n\ts_nop 0\n\tglobal_load_lds_dword %0, %1" ::"v"(c.vpf), "s"(c.pf), "s"(c.ldsD) : "memory");
  }
  WAIT_L0();
  BAR();
  k1_mma(acc, fb, fa);
  BAR();
  k1_load<BUF, 1>(lds, aB, aA, fb, fa);
  if (MODE == 0) { if (GRP == 0) k1_issue_B<BUF>(c); else k1_issue_A<BUF>(c); WAIT_V(9); }
  else if (MODE == 1) { WAIT_V(4); }
  WAIT_L0();
  BAR();
  k1_mma(acc, fb, fa);
  if (!(MODE == 2 && GRP == 1)) BAR();
}
template <int GRP>
DI void k1_main(LAS char* lds, K1Ctx& c, const unsigned (&aB)[10], const unsigned (&aA)[2], f32x4 (&acc)[10][4]) {
  if (GRP == 1) BAR();
#pragma unroll 1
  for (int t = 0; t < 14; t += 2) {
    k1_tile<GRP, 0, 0>(lds, c, aB, aA, acc);
    k1_tile<GRP, 1, 0>(lds, c, aB, aA, acc);
  }
  k1_tile<GRP, 0, 1>(lds, c, aB, aA, acc);
  k1_tile<GRP, 1, 2>(lds, c, aB, aA, acc);
  MFMA_DRAIN();
}

DI void epi_h(const f32x4 (&ga)[4], const f32x4 (&ua)[4], char* pp) {
#pragma unroll
  for (int mp = 0; mp < 2; ++mp) {
    u32x2 w[2];
#pragma unroll
    for (int h = 0; h < 2; ++h) {
      const int m = 2 * mp + h;
      w[h][0] = gelu_mul2(ga[m].xy, ua[m].xy);
      w[h][1] = gelu_mul2(ga[m].zw, ua[m].zw);
    }
    *(u32x4*)(pp + mp * 1024) = widen(w[0], w[1]);
  }
}
DI void epi_q(const f32x4 (&a0)[4], const f32x4 (&a1)[4], char* pp) {
#pragma unroll
  for (int mp = 0; mp < 2; ++mp) {
    *(u32x4*)(pp + mp * 1024) = widen(pk4(a0[2 * mp]), pk4(a0[2 * mp + 1]));
    *(u32x4*)(pp + PANEL + mp * 1024) = widen(pk4(a1[2 * mp]), pk4(a1[2 * mp + 1]));
  }
}
DI void epi_kv(const bf16x8 (&kp)[4], const bf16x8 (&vp)[4], LAS char* wphi  ,
               LAS char* scr  , LAS char* red  , int lane) {
  const int g = lane >> 4, c = lane & 15, q = (lane >> 2) & 3, p = lane & 3;
  bf16x8 wf[2];
#pragma unroll
  for (int fb = 0; fb < 2; ++fb) {
    LAS char* a = wphi + (4 * g + q) * 64 + fb * 32 + p * 8;
    s16x4 lo = __builtin_amdgcn_ds_read_tr16_b64_v4i16((LAS s16x4*)a);
    s16x4 hi = __builtin_amdgcn_ds_read_tr16_b64_v4i16((LAS s16x4*)(a + 1024));
    wf[fb] = __builtin_shufflevector(lo, hi, 0, 1, 2, 3, 4, 5, 6, 7);
  }
  const u32x4 onesu = {0x3F803F80u, 0x3F803F80u, 0x3F803F80u, 0x3F803F80u};
  const bf16x8 ONES = __builtin_bit_cast(bf16x8, onesu);
  const f32x4 Z = {0.f, 0.f, 0.f, 0.f};
  LAS char* wr = scr + c * 200 + g * 8;
  LAS char* rd = scr + (8 * g + q) * 200 + p * 8;
  f32x4 kv[4][3];
#pragma unroll
  for (int a = 0; a < 4; ++a)
#pragma unroll
    for (int b = 0; b < 3; ++b) kv[a][b] = Z;
#pragma unroll
  for (int mp = 0; mp < 2; ++mp) {
    f32x4 pp[2][2];
#pragma unroll
    for (int h = 0; h < 2; ++h) { pp[h][0] = MFMA(wf[0], kp[2 * mp + h], Z); pp[h][1] = MFMA(wf[1], kp[2 * mp + h], Z); }
    bf16x8 fp[2], fn[2];
    feature_map2(pp, fp, fn);
#pragma unroll
    for (int h = 0; h < 2; ++h) {
      const u32x4 a = __builtin_bit_cast(u32x4, fp[h]), b = __builtin_bit_cast(u32x4, fn[h]), v = __builtin_bit_cast(u32x4, vp[2 * mp + h]);
      LAS char* w = wr + h * 3200;
      *(LAS u32x2*)(w) = a.xy;       *(LAS u32x2*)(w + 32) = a.zw;
      *(LAS u32x2*)(w + 64) = b.xy;  *(LAS u32x2*)(w + 96) = b.zw;
      *(LAS u32x2*)(w + 128) = v.xy; *(LAS u32x2*)(w + 160) = v.zw;
    }
    bf16x8 fr[6];
#pragma unroll
    for (int a = 0; a < 6; ++a) {
      s16x4 lo = __builtin_amdgcn_ds_read_tr16_b64_v4i16((LAS s16x4*)(rd + a * 32));
      s16x4 hi = __builtin_amdgcn_ds_read_tr16_b64_v4i16((LAS s16x4*)(rd + a * 32 + 800));
      fr[a] = __builtin_shufflevector(lo, hi, 0, 1, 2, 3, 4, 5, 6, 7);
    }
#pragma unroll
    for (int a = 0; a < 4; ++a) {
      kv[a][0] = MFMA(fr[a], fr[4], kv[a][0]);
      kv[a][1] = MFMA(fr[a], fr[5], kv[a][1]);
      kv[a][2] = MFMA(fr[a], ONES, kv[a][2]);
    }
  }
#pragma unroll
  for (int a = 0; a < 4; ++a) {
    *(LAS f32x4*)(red + (a * 2 + 0) * 1024 + lane * 16) = kv[a][0];
    *(LAS f32x4*)(red + (a * 2 + 1) * 1024 + lane * 16) = kv[a][1];
    if (c == 0) *(LAS f32x4*)(red + 8192 + a * 64 + g * 16) = kv[a][2];
  }
}

__global__ __launch_bounds__(512, 2) void k1_kernel(const unsigned short* __restrict__ x, const unsigned short* __restrict__ wqkv,
                                                    const unsigned short* __restrict__ wphi, const unsigned short* __restrict__ wg,
                                                    const unsigned short* __restrict__ wu, const unsigned short* __restrict__ gatew,
                                                    char* __restrict__ act, char* __restrict__ qbuf,
                                                    float* __restrict__ kvp, float* __restrict__ gate, unsigned* __restrict__ flags,
                                                    const char* __restrict__ wd, const char* __restrict__ wo) {
  extern __shared__ __attribute__((aligned(16))) char smem[];
  LAS char* lds = (LAS char*)smem;
  const unsigned ldsb = (unsigned)(size_t)lds;
  const int tid = threadIdx.x, lane = tid & 63;
  const int wid = __builtin_amdgcn_readfirstlane(tid >> 6);
  const int grp = wid >> 2, j = wid & 3, wm = 2 * grp + (j >> 1), wn = (j & 1) ^ grp;
  const int bid = blockIdx.x, xcd = bid & 7, idx = bid >> 3;
  const int rt = idx & 7, cg = xcd * 4 + (idx >> 3);
  const int row0 = rt * 256;
  const int g = lane >> 4, c = lane & 15;

  STAMP_DECL;
  u32x4 gx0, gx1, gw0, gw1;
  {
    const char* xr = (const char*)x + (size_t)(bid * 8 + wid) * 2048;
    const unsigned vo = lane * 16;
    asm volatile("global_load_dwordx4 %0, %4, %5\n\tglobal_load_dwordx4 %1, %4, %5 offset:1024\n\t"
                 "global_load_dwordx4 %2, %4, %6\n\tglobal_load_dwordx4 %3, %4, %6 offset:1024"
                 : "=&v"(gx0), "=&v"(gx1), "=&v"(gw0), "=&v"(gw1) : "v"(vo), "s"(xr), "s"(gatew) : "memory");
  }

  K1Ctx cx;
  {
    const int ar = (grp == 0 ? 128 : 0) + 32 * j;
    cx.sA = (const char*)x + (unsigned)(row0 + ar) * 2048u;
    cx.ldsA = ldsb + K1_A0 + ar * 128;
    const int r = lane >> 3, ch = lane & 7;
    cx.voffA0 = r * 2048 + ((ch ^ (r >> 1)) * 16);
    cx.voffA1 = cx.voffA0 ^ 64;
    const unsigned kr = grp * 32 + 8 * j;
    const int l32 = (ch >> 1) ^ (((r >> 1) & 1) | ((j & 1) << 1));
    const unsigned h16 = (ch & 1) * 16;
    cx.sG = (const char*)wg + kr * SW + cg * 224;
    cx.sU = (const char*)wu + kr * SW + cg * 224;
    cx.sQ = (const char*)wqkv + kr * SQ + cg * 64;
    cx.voffW = r * SW + l32 * 32 + h16;
    cx.voffQ = r * SQ + (l32 < 2 ? 4096 + l32 * 32 : (l32 - 2) * 32) + h16;
    cx.vP2 = (l32 < 3 ? cx.sG + 128 + l32 * 32 : cx.sU + 128) + (r * SW + h16);
    cx.vP3 = l32 < 2 ? cx.sU + (160 + l32 * 32 + r * SW + h16) : cx.sQ + (2048 + (l32 - 2) * 32 + r * SQ + h16);
    cx.stepP3 = l32 < 2 ? 64 * SW : 64 * SQ;
    cx.ldsB = ldsb + K1_B0 + kr * 128;
    const int id = wid * 256 + bid;
    cx.pf = id < 896 ? wd + (size_t)id * 8192 : (id < 1152 ? wo + (size_t)(id - 896) * 8192 : nullptr);
    cx.ldsD = ldsb + K1_DUMP + wid * 256;
    cx.vpf = lane * 128u;
    cx.sA = uni_ptr(cx.sA); cx.sG = uni_ptr(cx.sG); cx.sU = uni_ptr(cx.sU); cx.sQ = uni_ptr(cx.sQ);
    asm volatile("s_nop 4" : "+s"(cx.sA), "+s"(cx.sG), "+s"(cx.sU), "+s"(cx.sQ));
  }
  if (wid < 2) glds_s(lane * 16, (const char*)wphi + cg * 2048 + wid * 1024, ldsb + K1_WPHI + wid * 1024);
  if (grp == 0) { k1_issue_B<0>(cx); k1_issue_A<0>(cx); k1_issue_B<1>(cx); }
  else          { k1_issue_A<0>(cx); k1_issue_B<0>(cx); k1_issue_A<1>(cx); }

  unsigned aB[10], aA[2];
  {
    const int q = (lane >> 2) & 3, p = lane & 3;
    const int swz = ((q >> 1) & 1) | ((g & 1) << 1);
    const unsigned lb = K1_B0 + (8 * g + q) * 128 + p * 8;
    const unsigned long long tb = wn ? k1_tab(1) : k1_tab(0);
#pragma unroll
    for (int i = 0; i < 10; ++i) {
      const unsigned code = (unsigned)(tb >> (5 * i)) & 31u;
      aB[i] = lb + (code >> 2) * 8192 + (((code & 3) ^ swz) * 32);
    }
    const int sa = (c >> 1) & 7;
    aA[0] = (64 * wm + c) * 128 + ((g ^ sa) * 16);
    aA[1] = aA[0] ^ 64;
  }

  {
    asm volatile("s_waitcnt vmcnt(13)" : "+v"(gx0), "+v"(gx1), "+v"(gw0), "+v"(gw1)::"memory");
    float s = 0.f;
#pragma unroll
    for (int e = 0; e < 4; ++e) {
      s = __builtin_fmaf(__uint_as_float(gx0[e] << 16), __uint_as_float(gw0[e] << 16), s);
      s = __builtin_fmaf(__uint_as_float(gx0[e] & 0xffff0000u), __uint_as_float(gw0[e] & 0xffff0000u), s);
      s = __builtin_fmaf(__uint_as_float(gx1[e] << 16), __uint_as_float(gw1[e] << 16), s);
      s = __builtin_fmaf(__uint_as_float(gx1[e] & 0xffff0000u), __uint_as_float(gw1[e] & 0xffff0000u), s);
    }
#pragma unroll
    for (int o = 1; o < 64; o <<= 1) s += __shfl_xor(s, o);
    const float gv = 1.0f / (1.0f + __expf(-s));
    if (lane == 0) {
      float* gp = gate + bid * 8 + wid;
      asm volatile("global_store_dword %0, %1, %2" ::"v"(0u), "v"(gv), "s"(gp) : "memory");
    }
  }

  f32x4 acc[10][4];
#pragma unroll
  for (int i = 0; i < 10; ++i)
#pragma unroll
    for (int m = 0; m < 4; ++m) acc[i][m] = (f32x4){0.f, 0.f, 0.f, 0.f};

  WAIT_V(9);
  BAR();
  STAMP(1);
  if (grp == 0) k1_main<0>(lds, cx, aB, aA, acc); else k1_main<1>(lds, cx, aB, aA, acc);

  STAMP(2);
  int tz = threadIdx.x;
  asm volatile("" : "+v"(tz));
  const int lane2 = tz & 63, g2 = lane2 >> 4;
  if (tz < 8) flags[(16 + bid * 8 + tz) * 16] = 0u;
  if (bid == 0 && tz >= 64 && tz < 72) flags[(tz - 64) * 16] = 0u;
  const unsigned lo = (unsigned)(row0 + 64 * wm + 16 * (g2 & 1) + (lane2 & 15)) * 32u + (g2 >> 1) * 16u;
  char* hp = act + (size_t)(cg * 7 + 5 * wn) * PANEL + lo;
  epi_h(acc[0], acc[1], hp);
  epi_h(acc[2], acc[3], hp + PANEL);
  STAMP(3);
  if (wn == 0) {
    epi_h(acc[4], acc[5], hp + 2 * PANEL);
    epi_h(acc[6], acc[7], hp + 3 * PANEL);
    epi_h(acc[8], acc[9], hp + 4 * PANEL);
  } else {
    epi_q(acc[8], acc[9], qbuf + (size_t)(2 * cg) * PANEL + lo);
    bf16x8 kp[4], vp[4];
#pragma unroll
    for (int m = 0; m < 4; ++m) { kp[m] = pack8(acc[4][m], acc[5][m]); vp[m] = pack8(acc[6][m], acc[7][m]); }
    epi_kv(kp, vp, lds + K1_WPHI, lds + 4 * 8448 + wm * 6400, lds + wm * 8448, lane2);
  }
  STAMP(4);
  __syncthreads();
  STAMP(5);
  for (int e = tz; e < 528; e += 512) {
    LAS char* p = lds + e * 16;
    const f32x4 s = (*(LAS f32x4*)p + *(LAS f32x4*)(p + 8448)) + (*(LAS f32x4*)(p + 2 * 8448) + *(LAS f32x4*)(p + 3 * 8448));
    *(f32x4*)(kvp + ((size_t)rt * 32 + cg) * KVP + e * 4) = s;
  }
  STAMP(6);
  STAMP_OUT((char*)gate + (1u << 20), wn);
}

constexpr int K2_ST = 32768, K2_NST = 4, K2_AUX = K2_ST * K2_NST, K2_LDS = K2_AUX + 8192;
constexpr int FL_OCNT = 0;
constexpr int FL_X = 16;
#define RLX_AGENT __ATOMIC_RELAXED, __HIP_MEMORY_SCOPE_AGENT

DI void glds_s1(unsigned voff, const void* sbase, unsigned ldsaddr) {
  asm volatile("s_mov_b32 m0, %2\n\ts_nop 0\n\tglobal_load_lds_dwordx4 %0, %1 sc1" ::"v"(voff), "s"(sbase), "s"(ldsaddr) : "memory");
}
DI void st16_sc1(void* p, u32x4 v) { asm volatile("global_store_dwordx4 %0, %1, off sc1\n\ts_nop 1" ::"v"(p), "v"(v) : "memory"); }
DI void wait_ge(const unsigned* p, unsigned need) {
  for (unsigned spins = 0; spins < (1u << 22); ++spins) {
    if ((unsigned)__builtin_amdgcn_readfirstlane(__hip_atomic_load(p, RLX_AGENT)) >= need) break;
    __builtin_amdgcn_s_sleep(1);
  }
}

DI f32x4 sel4(int cnd, f32x4 a, f32x4 b) { return cnd ? a : b; }

struct K2Ctx {
  const char* sA; const char* sB;
  unsigned voffB, vlin;
  unsigned ldsA, ldsB;
};
template <bool SC1> DI void k2_issue(K2Ctx& c, int slot) {
  const unsigned o = slot * K2_ST;
  if (SC1) { glds_s1(c.vlin, c.sA, c.ldsA + o); glds_s1(c.vlin, c.sA + 1024, c.ldsA + o + 1024); }
  else     { glds_s(c.vlin, c.sA, c.ldsA + o); glds_s(c.vlin, c.sA + 1024, c.ldsA + o + 1024); }
  glds_s(c.voffB, c.sB, c.ldsB + o);
  glds_s(c.voffB, c.sB + 128, c.ldsB + o + 8192);
  c.sA += 4 * PANEL;
  c.sB += 64 * 2048;
}
template <int SLOT> DI void k2_load(LAS char* lds, unsigned aA, const unsigned (&aB)[4], bf16x8 (&fb)[4], bf16x8 (&fa)[4]) {
#pragma unroll
  for (int m = 0; m < 4; ++m) fa[m] = *(LAS bf16x8*)(lds + aA + ((SLOT & 1) * K2_ST + m * 512));
#pragma unroll
  for (int i = 0; i < 4; ++i) fb[i] = tr_frag(lds + aB[i], (SLOT & 1) * K2_ST);
}
DI void k2_mma(f32x4 (&acc)[4][4], const bf16x8 (&fb)[4], const bf16x8 (&fa)[4]) {
  __builtin_amdgcn_s_setprio(1);
#pragma unroll
  for (int i = 0; i < 4; ++i)
#pragma unroll
    for (int m = 0; m < 4; ++m) MFMA_ACC(acc[i][m], fb[i], fa[m]);
  __builtin_amdgcn_s_setprio(0);
}
template <int SLOT, int ISSUE, int WAITN, bool LASTBAR>
DI void k2_step(LAS char* lds, K2Ctx& c, const unsigned (&aA)[2], const unsigned (&aB)[2][4], f32x4 (&acc)[4][4]) {
  bf16x8 fb[4], fa[4];
  k2_load<SLOT>(lds, aA[SLOT >> 1], aB[SLOT >> 1], fb, fa);
  if (ISSUE == 1) k2_issue<false>(c, (SLOT + 3) & 3);
  if (ISSUE == 2) k2_issue<true>(c, (SLOT + 3) & 3);
  asm volatile("s_waitcnt vmcnt(%0)" ::"i"(WAITN) : "memory");
  WAIT_L0();
  BAR();
  k2_mma(acc, fb, fa);
  if (LASTBAR) BAR();
}
template <int ISSUE> DI void k2_step4(LAS char* lds, K2Ctx& c, const unsigned (&aA)[2], const unsigned (&aB)[2][4], f32x4 (&acc)[4][4]) {
  k2_step<0, ISSUE, 8, true>(lds, c, aA, aB, acc);
  k2_step<1, ISSUE, 8, true>(lds, c, aA, aB, acc);
  k2_step<2, ISSUE, 8, true>(lds, c, aA, aB, acc);
  k2_step<3, ISSUE, 8, true>(lds, c, aA, aB, acc);
}

__global__ __launch_bounds__(512, 2) void k2_kernel(char* __restrict__ act, const char* __restrict__ qbuf,
                                                    const unsigned short* __restrict__ wphi, const char* __restrict__ wd,
                                                    const char* __restrict__ wo, const float* __restrict__ kvp,
                                                    const float* __restrict__ gate, char* __restrict__ xbuf,
                                                    unsigned* __restrict__ flags, unsigned short* __restrict__ out) {
  extern __shared__ __attribute__((aligned(16))) char smem[];
  LAS char* lds = (LAS char*)smem;
  const unsigned ldsb = (unsigned)(size_t)lds;
  const int tid = threadIdx.x, lane = tid & 63;
  const int wid = __builtin_amdgcn_readfirstlane(tid >> 6);
  const int grp = wid >> 2, j = wid & 3, wm = j >> 1, wn = j & 1;
  const int bid = blockIdx.x, xcd = bid & 7, idx = bid >> 3;
  const int ks = xcd >> 2, qd = xcd & 3;
  const int tm = (qd >> 1) * 8 + (idx & 7), tn = (qd & 1) * 4 + (idx >> 3);
  const int g = lane >> 4, c = lane & 15, q = (lane >> 2) & 3, p = lane & 3;

  STAMP_DECL;
  K2Ctx cx;
  {
    const int r = lane >> 3, ch = lane & 7;
    const int kb = 2 * grp + (j >> 1);
    cx.sA = act + (size_t)(ks * 144 + kb) * PANEL + (tm * 128 + (j & 1) * 64) * 32;
    cx.ldsA = ldsb + kb * 4096 + (j & 1) * 2048;
    cx.vlin = lane * 16;
    const int kr = 32 * grp + 8 * j;
    cx.sB = wd + (size_t)(ks * 2304 + kr) * 2048 + tn * 256;
    cx.voffB = r * 2048 + (((ch >> 1) ^ (((r >> 1) & 1) | ((j & 1) << 1))) * 32) + (ch & 1) * 16;
    cx.ldsB = ldsb + 16384 + kr * 128;
  }
  const int head = bid & 31, rb = bid >> 5;
  float og[4];
  {
    LAS char* aux = lds + K2_AUX;
    const char* kvb = (const char*)kvp + (size_t)head * (KVP * 4);
    constexpr unsigned RS = 32u * KVP * 4u;
    f32x4 zq[8], kq[8];
    if (wid == 0) {
      const unsigned vo = (512 + c) * 16;
      asm volatile("global_load_dwordx4 %0, %8, %9\n\tglobal_load_dwordx4 %1, %8, %10\n\tglobal_load_dwordx4 %2, %8, %11\n\tglobal_load_dwordx4 %3, %8, %12\n\t"
                   "global_load_dwordx4 %4, %8, %13\n\tglobal_load_dwordx4 %5, %8, %14\n\tglobal_load_dwordx4 %6, %8, %15\n\tglobal_load_dwordx4 %7, %8, %16"
                   : "=&v"(zq[0]), "=&v"(zq[1]), "=&v"(zq[2]), "=&v"(zq[3]), "=&v"(zq[4]), "=&v"(zq[5]), "=&v"(zq[6]), "=&v"(zq[7])
                   : "v"(vo), "s"(kvb), "s"(kvb + RS), "s"(kvb + 2 * RS), "s"(kvb + 3 * RS), "s"(kvb + 4 * RS), "s"(kvb + 5 * RS), "s"(kvb + 6 * RS), "s"(kvb + 7 * RS)
                   : "memory");
    } else if (wid <= 2) {
      glds_s(lane * 16, (const char*)wphi + (size_t)head * 2048 + (wid - 1) * 1024, ldsb + K2_AUX + 528 * 8 + (wid - 1) * 1024);
    }
    {
      const unsigned vo = tid * 16;
      asm volatile("global_load_dwordx4 %0, %8, %9\n\tglobal_load_dwordx4 %1, %8, %10\n\tglobal_load_dwordx4 %2, %8, %11\n\tglobal_load_dwordx4 %3, %8, %12\n\t"
                   "global_load_dwordx4 %4, %8, %13\n\tglobal_load_dwordx4 %5, %8, %14\n\tglobal_load_dwordx4 %6, %8, %15\n\tglobal_load_dwordx4 %7, %8, %16"
                   : "=&v"(kq[0]), "=&v"(kq[1]), "=&v"(kq[2]), "=&v"(kq[3]), "=&v"(kq[4]), "=&v"(kq[5]), "=&v"(kq[6]), "=&v"(kq[7])
                   : "v"(vo), "s"(kvb), "s"(kvb + RS), "s"(kvb + 2 * RS), "s"(kvb + 3 * RS), "s"(kvb + 4 * RS), "s"(kvb + 5 * RS), "s"(kvb + 6 * RS), "s"(kvb + 7 * RS)
                   : "memory");
    }
    bf16x8 qf[2];
    float gt[2];
    {
      const char* qb = qbuf + (size_t)(2 * head) * PANEL + (rb * 256 + wid * 32) * 32;
      const unsigned vq = (g >> 1) * PANEL + c * 32 + (g & 1) * 16, vg = c * 4;
      const float* g0 = gate + rb * 256 + wid * 32;
      const float* g1 = gate + tm * 128 + 64 * wm;
      asm volatile("global_load_dwordx4 %0, %8, %10\n\tglobal_load_dwordx4 %1, %8, %10 offset:512\n\t"
                   "global_load_dword %2, %9, %11\n\tglobal_load_dword %3, %9, %11 offset:64\n\t"
                   "global_load_dword %4, %9, %12\n\tglobal_load_dword %5, %9, %12 offset:64\n\t"
                   "global_load_dword %6, %9, %12 offset:128\n\tglobal_load_dword %7, %9, %12 offset:192"
                   : "=&v"(qf[0]), "=&v"(qf[1]), "=&v"(gt[0]), "=&v"(gt[1]), "=&v"(og[0]), "=&v"(og[1]), "=&v"(og[2]), "=&v"(og[3])
                   : "v"(vq), "v"(vg), "s"(qb), "s"(g0), "s"(g1) : "memory");
    }
    k2_issue<false>(cx, 0); k2_issue<false>(cx, 1); k2_issue<false>(cx, 2);
    asm volatile("s_waitcnt vmcnt(20)" : "+v"(kq[0]), "+v"(kq[1]), "+v"(kq[2]), "+v"(kq[3]), "+v"(kq[4]), "+v"(kq[5]), "+v"(kq[6]), "+v"(kq[7])::"memory");
    *(LAS u32x2*)(aux + tid * 8) = pk4(((kq[0] + kq[1]) + (kq[2] + kq[3])) + ((kq[4] + kq[5]) + (kq[6] + kq[7])));
    if (wid == 0) {
      asm volatile("" : "+v"(zq[0]), "+v"(zq[1]), "+v"(zq[2]), "+v"(zq[3]), "+v"(zq[4]), "+v"(zq[5]), "+v"(zq[6]), "+v"(zq[7]));
      *(LAS u32x2*)(aux + (512 + c) * 8) = pk4(((zq[0] + zq[1]) + (zq[2] + zq[3])) + ((zq[4] + zq[5]) + (zq[6] + zq[7])));
    }
    asm volatile("s_waitcnt vmcnt(12)" : "+v"(qf[0]), "+v"(qf[1]), "+v"(gt[0]), "+v"(gt[1]), "+v"(og[0]), "+v"(og[1]), "+v"(og[2]), "+v"(og[3])::"memory");
    WAIT_L0();
    BAR();
    bf16x8 wf[2];
#pragma unroll
    for (int fb = 0; fb < 2; ++fb) {
      LAS char* a = aux + 528 * 8 + (8 * g + q) * 64 + fb * 32 + p * 8;
      s16x4 lo = __builtin_amdgcn_ds_read_tr16_b64_v4i16((LAS s16x4*)a);
      s16x4 hi = __builtin_amdgcn_ds_read_tr16_b64_v4i16((LAS s16x4*)(a + 256));
      wf[fb] = __builtin_shufflevector(lo, hi, 0, 1, 2, 3, 4, 5, 6, 7);
    }
    bf16x8 kf[2][2], zf[2];
#pragma unroll
    for (int h = 0; h < 2; ++h) {
#pragma unroll
      for (int db = 0; db < 2; ++db) {
        const u32x2 lo = *(LAS u32x2*)(aux + (((2 * h) * 2 + db) * 64 + lane) * 8);
        const u32x2 hi = *(LAS u32x2*)(aux + (((2 * h + 1) * 2 + db) * 64 + lane) * 8);
        const u32x4 w = {lo[0], lo[1], hi[0], hi[1]};
        kf[h][db] = __builtin_bit_cast(bf16x8, w);
      }
      const u32x2 lo = *(LAS u32x2*)(aux + (512 + (2 * h) * 4 + g) * 8);
      const u32x2 hi = *(LAS u32x2*)(aux + (512 + (2 * h + 1) * 4 + g) * 8);
      const u32x4 w = {lo[0], lo[1], hi[0], hi[1]};
      zf[h] = __builtin_bit_cast(bf16x8, w);
    }
    const f32x4 Z = {0.f, 0.f, 0.f, 0.f};
    f32x4 pp[2][2];
#pragma unroll
    for (int m = 0; m < 2; ++m) { pp[m][0] = MFMA(wf[0], qf[m], Z); pp[m][1] = MFMA(wf[1], qf[m], Z); }
    bf16x8 fp[2], fn[2];
    feature_map2(pp, fp, fn);
    u32x2 w0[2], w1[2];
#pragma unroll
    for (int m = 0; m < 2; ++m) {
      f32x4 n0 = MFMA(kf[0][0], fp[m], Z); n0 = MFMA(kf[1][0], fn[m], n0);
      f32x4 n1 = MFMA(kf[0][1], fp[m], Z); n1 = MFMA(kf[1][1], fn[m], n1);
      f32x4 dn = MFMA(zf[0], fp[m], Z); dn = MFMA(zf[1], fn[m], dn);
      const float sc = gt[m] / (dn[0] + 1e-6f);
      w0[m] = pk4(n0 * sc); w1[m] = pk4(n1 * sc);
    }
    char* op = act + (size_t)(224 + 2 * head) * PANEL + (rb * 256 + wid * 32 + 16 * (g & 1) + c) * 32 + (g >> 1) * 16;
    st16_sc1(op, widen(w0[0], w0[1]));
    st16_sc1(op + PANEL, widen(w1[0], w1[1]));
  }

  STAMP(1);
  unsigned aA[2], aB[2][4];
  {
    aA[0] = (2 * grp + (g >> 1)) * 4096 + (64 * wm + c) * 32 + (g & 1) * 16;
    aA[1] = aA[0] + 2 * K2_ST;
    const int swz = ((q >> 1) & 1) | ((g & 1) << 1);
#pragma unroll
    for (int i = 0; i < 4; ++i) {
      aB[0][i] = 16384 + wn * 8192 + (32 * grp + 8 * g + q) * 128 + p * 8 + ((i ^ swz) * 32);
      aB[1][i] = aB[0][i] + 2 * K2_ST;
    }
  }
#pragma unroll
  for (int m = 0; m < 4; ++m) og[m] = 1.0f - og[m];
  f32x4 acc[4][4];
#pragma unroll
  for (int i = 0; i < 4; ++i)
#pragma unroll
    for (int m = 0; m < 4; ++m) acc[i][m] = (f32x4){0.f, 0.f, 0.f, 0.f};

  WAIT_V(10);
  BAR();
  if (grp == 1) BAR();
#pragma unroll 1
  for (int t = 0; t < 16; t += 4) {
    if (t == 4 && tid == 0) __hip_atomic_fetch_add(flags + (FL_OCNT + rb) * 16, 1u, RLX_AGENT);
    k2_step4<1>(lds, cx, aA, aB, acc);
  }
  k2_step<0, 1, 8, false>(lds, cx, aA, aB, acc);
  STAMP(2);
  if (ks == 1) {
    if (wid == 0) {
      wait_ge(flags + (FL_OCNT + (tm >> 1)) * 16, 32u);
    }
    cx.sB = wo + (32 * grp + 8 * j) * 2048 + tn * 256;
  }
  BAR();
  STAMP(3);
  k2_step<1, 2, 8, true>(lds, cx, aA, aB, acc);
  k2_step<2, 2, 8, true>(lds, cx, aA, aB, acc);
  k2_step<3, 2, 8, true>(lds, cx, aA, aB, acc);
  if (ks == 1) {
    MFMA_DRAIN();
#pragma unroll
    for (int i = 0; i < 4; ++i)
#pragma unroll
      for (int m = 0; m < 4; ++m) acc[i][m] *= og[m];
    SCHED(); asm volatile("s_nop 1"); SCHED();
  }
#pragma unroll 1
  for (int t = 20; t < 32; t += 4) k2_step4<2>(lds, cx, aA, aB, acc);
  k2_step<0, 2, 8, true>(lds, cx, aA, aB, acc);
  k2_step<1, 0, 4, true>(lds, cx, aA, aB, acc);
  k2_step<2, 0, 0, true>(lds, cx, aA, aB, acc);
  k2_step<3, 0, 0, false>(lds, cx, aA, aB, acc);
  STAMP(4);
  if (grp == 0) BAR();
  MFMA_DRAIN();
  if (ks == 0) {
#pragma unroll
    for (int i = 0; i < 4; ++i)
#pragma unroll
      for (int m = 0; m < 4; ++m) acc[i][m] *= og[m];
  }

  f32x4 mine[2][4];
  {
    LAS char* wr = lds + wid * 8192 + lane * 16;
    LAS char* rd = lds + (wid ^ 4) * 8192 + lane * 16;
#pragma unroll
    for (int i = 0; i < 2; ++i)
#pragma unroll
      for (int m = 0; m < 4; ++m) {
        *(LAS f32x4*)(wr + (i * 4 + m) * 1024) = sel4(grp, acc[i][m], acc[2 + i][m]);
        mine[i][m] = sel4(grp, acc[2 + i][m], acc[i][m]);
      }
    __syncthreads();
#pragma unroll
    for (int i = 0; i < 2; ++i)
#pragma unroll
      for (int m = 0; m < 4; ++m) mine[i][m] += *(LAS f32x4*)(rd + (i * 4 + m) * 1024);
  }
  STAMP(5);
  {
    char* xs = xbuf + ((size_t)bid * 8 + wid) * 4096 + lane * 16;
#pragma unroll
    for (int i = 0; i < 2; ++i)
#pragma unroll
      for (int m = 0; m < 2; ++m)
        st16_sc1(xs + (i * 2 + m) * 1024, __builtin_bit_cast(u32x4, sel4(ks, mine[i][m], mine[i][2 + m])));
    WAIT_V(0);
    if (lane == 0) __hip_atomic_store(flags + (FL_X + bid * 8 + wid) * 16, 1u, RLX_AGENT);
    wait_ge(flags + (FL_X + (bid ^ 4) * 8 + wid) * 16, 1u);
    __builtin_amdgcn_fence(__ATOMIC_ACQUIRE, "wavefront");
    STAMP(6);
    const char* xr = xbuf + ((size_t)(bid ^ 4) * 8 + wid) * 4096 + lane * 16;
    f32x4 o[4];
    asm volatile("global_load_dwordx4 %0, %4, off sc1\n\tglobal_load_dwordx4 %1, %4, off offset:1024 sc1\n\t"
                 "global_load_dwordx4 %2, %4, off offset:2048 sc1\n\tglobal_load_dwordx4 %3, %4, off offset:3072 sc1\n\t"
                 "s_waitcnt vmcnt(0)"
                 : "=&v"(o[0]), "=&v"(o[1]), "=&v"(o[2]), "=&v"(o[3]) : "v"(xr) : "memory");
#pragma unroll
    for (int m = 0; m < 2; ++m) {
      const f32x4 v0 = sel4(ks, mine[0][2 + m], mine[0][m]) + o[m];
      const f32x4 v1 = sel4(ks, mine[1][2 + m], mine[1][m]) + o[2 + m];
      const u32x4 w = widen(pk4(v0), pk4(v1));
      unsigned short* op = out + (size_t)(tm * 128 + 64 * wm + 32 * ks + 16 * m + c) * D_ + tn * 128 + 64 * wn + 32 * grp + 16 * (g & 1) + 8 * (g >> 1);
      *(u32x4*)op = w;
    }
  }
  STAMP_OUT((char*)gate + (2u << 20), ks);
}

extern "C" void kernel_launch(void* const* d_in, const int* in_sizes, int n_in, void* d_out, int out_size, void* d_ws,
                              size_t ws_size, hipStream_t stream) {
  const unsigned short* x = (const unsigned short*)d_in[0];
  const unsigned short* wqkv = (const unsigned short*)d_in[1];
  const unsigned short* wphi = (const unsigned short*)d_in[2];
  const char* wo = (const char*)d_in[3];
  const unsigned short* wg = (const unsigned short*)d_in[5];
  const unsigned short* wu = (const unsigned short*)d_in[6];
  const char* wd = (const char*)d_in[7];
  const unsigned short* gatew = (const unsigned short*)d_in[8];
  char* ws = (char*)d_ws;
  char* act = ws;
  char* qbuf = ws + (18u << 20);
  float* kvp = (float*)(ws + (22u << 20));
  char* xbuf = ws + (25u << 20) ;
  unsigned* flags = (unsigned*)(ws + (33u << 20));
  float* gate = (float*)(ws + (34u << 20));
  (void)hipFuncSetAttribute((const void*)k1_kernel, hipFuncAttributeMaxDynamicSharedMemorySize, K1_LDS);
  (void)hipFuncSetAttribute((const void*)k2_kernel, hipFuncAttributeMaxDynamicSharedMemorySize, K2_LDS);
  k1_kernel<<<256, 512, K1_LDS, stream>>>(x, wqkv, wphi, wg, wu, gatew, act, qbuf, kvp, gate, flags, wd, wo);
  k2_kernel<<<256, 512, K2_LDS, stream>>>(act, qbuf, wphi, wd, wo, kvp, gate, xbuf, flags, (unsigned short*)d_out);
}
```

```cpp
#include <hip/hip_runtime.h>
#include <hip/hip_bf16.h>
#include <stdint.h>

typedef short bf16x8 __attribute__((ext_vector_type(8)));
typedef short s16x4 __attribute__((ext_vector_type(4)));
typedef float f32x4 __attribute__((ext_vector_type(4)));
typedef float f32x2 __attribute__((ext_vector_type(2)));
typedef unsigned u32x4 __attribute__((ext_vector_type(4)));
typedef unsigned u32x2 __attribute__((ext_vector_type(2)));
typedef __bf16 bf16x2_t __attribute__((ext_vector_type(2)));
#define LAS __attribute__((address_space(3)))
#define GAS __attribute__((address_space(1)))
#define DI __device__ __forceinline__

constexpr int S_ = 2048, D_ = 1024, F_ = 3584;
constexpr int PANEL = S_ * 32;
constexpr int KVP = 2112;

#define STAMP_DECL
#define STAMP(i)
#define STAMP_OUT(base, tag)
DI void glds_s(unsigned voff, const void* sbase, unsigned ldsaddr) {
  asm volatile("s_mov_b32 m0, %2\n\ts_nop 0\n\tglobal_load_lds_dwordx4 %0, %1" ::"v"(voff), "s"(sbase), "s"(ldsaddr) : "memory");
}
DI void glds_v(const void* vaddr, unsigned ldsaddr) {
  asm volatile("s_mov_b32 m0, %1\n\ts_nop 0\n\tglobal_load_lds_dwordx4 %0, off" ::"v"(vaddr), "s"(ldsaddr) : "memory");
}
DI const char* uni_ptr(const char* p) {
  const unsigned long long v = (unsigned long long)p;
  const unsigned lo = __builtin_amdgcn_readfirstlane((unsigned)v), hi = __builtin_amdgcn_readfirstlane((unsigned)(v >> 32));
  return (const char*)(((unsigned long long)hi << 32) | lo);
}
#define WAIT_V(n) asm volatile("s_waitcnt vmcnt(" #n ")" ::: "memory")
#define WAIT_L0() __builtin_amdgcn_s_waitcnt(0xC07F)
#define SCHED() __builtin_amdgcn_sched_barrier(0)
#define BAR() do { SCHED(); asm volatile("" ::: "memory"); __builtin_amdgcn_s_barrier(); asm volatile("" ::: "memory"); SCHED(); } while (0)
#define MFMA_ACC(c, a, b) asm volatile("v_mfma_f32_16x16x32_bf16 %0, %1, %2, %0" : "+v"(c) : "v"(a), "v"(b))
#define MFMA_DRAIN() do { SCHED(); asm volatile("s_nop 7\n\ts_nop 7" ::: "memory"); SCHED(); } while (0)
#define MFMA(a, b, c) __builtin_amdgcn_mfma_f32_16x16x32_bf16((a), (b), (c), 0, 0, 0)

DI unsigned pk2(float lo, float hi) {
  f32x2 v = {lo, hi};
  bf16x2_t r = __builtin_convertvector(v, bf16x2_t);
  return __builtin_bit_cast(unsigned, r);
}
DI bf16x8 pack8(const f32x4& a, const f32x4& b) {
  u32x4 p = {pk2(a[0], a[1]), pk2(a[2], a[3]), pk2(b[0], b[1]), pk2(b[2], b[3])};
  return __builtin_bit_cast(bf16x8, p);
}
DI bf16x8 tr_frag(LAS char* p, int off) {
  s16x4 a = __builtin_amdgcn_ds_read_tr16_b64_v4i16((LAS s16x4*)(p + off));
  s16x4 b = __builtin_amdgcn_ds_read_tr16_b64_v4i16((LAS s16x4*)(p + off + 512));
  return __builtin_shufflevector(a, b, 0, 1, 2, 3, 4, 5, 6, 7);
}
DI float bf2f(unsigned short u) { return __uint_as_float(((unsigned)u) << 16); }
DI unsigned gelu_mul2(f32x2 x, f32x2 u) {
  const float c0 = -2.3022081984f;
  const float c1 = -2.3022081984f * 0.044715f;
  const f32x2 p = __builtin_elementwise_fma(x * x, (f32x2){c1, c1}, (f32x2){c0, c0});
  const f32x2 a = x * p;
  const f32x2 e = {__builtin_amdgcn_exp2f(a[0]), __builtin_amdgcn_exp2f(a[1])};
  const f32x2 d = e + 1.0f;
  const f32x2 r = {__builtin_amdgcn_rcpf(d[0]), __builtin_amdgcn_rcpf(d[1])};
  const f32x2 o = (x * u) * r;
  return pk2(o[0], o[1]);
}
DI void swap16(float& a, float& b) {
  auto r = __builtin_amdgcn_permlane16_swap(__float_as_uint(a), __float_as_uint(b), false, false);
  a = __uint_as_float(r[0]); b = __uint_as_float(r[1]);
}
DI void swap32(float& a, float& b) {
  auto r = __builtin_amdgcn_permlane32_swap(__float_as_uint(a), __float_as_uint(b), false, false);
  a = __uint_as_float(r[0]); b = __uint_as_float(r[1]);
}
template <bool MAX> DI float rop(float a, float b) { return MAX ? fmaxf(a, b) : a + b; }
template <bool MAX> DI void xrow4(float& a, float& b, float& c, float& d) {
  swap32(a, b); float r1 = rop<MAX>(a, b);
  swap32(c, d); float r2 = rop<MAX>(c, d);
  swap16(r1, r2); float r = rop<MAX>(r1, r2);
  float u = r, v = r; swap16(u, v);
  a = u; b = u; swap32(a, b);
  c = v; d = v; swap32(c, d);
}
DI u32x4 widen(u32x2 a, u32x2 b) {
  auto r0 = __builtin_amdgcn_permlane16_swap(a[0], b[0], false, false);
  auto r1 = __builtin_amdgcn_permlane16_swap(a[1], b[1], false, false);
  u32x4 w = {r0[0], r1[0], r0[1], r1[1]};
  return w;
}
DI u32x2 pk4(const f32x4& v) { u32x2 w = {pk2(v[0], v[1]), pk2(v[2], v[3])}; return w; }

DI float max3(float a, float b, float c) { return fmaxf(fmaxf(a, b), c); }
DI float min3(float a, float b, float c) { return fminf(fminf(a, b), c); }
DI f32x4 exp4(const f32x4& p, float s, float o) {
  const f32x4 t = __builtin_elementwise_fma(p, (f32x4){s, s, s, s}, (f32x4){o, o, o, o});
  f32x4 e = {__builtin_amdgcn_exp2f(t[0]), __builtin_amdgcn_exp2f(t[1]), __builtin_amdgcn_exp2f(t[2]), __builtin_amdgcn_exp2f(t[3])};
  return e;
}
DI float sum8(const f32x4& a, const f32x4& b) { const f32x4 t = a + b; const f32x2 u = t.xy + t.zw; return u[0] + u[1]; }
DI void feature_map2(const f32x4 (&p)[2][2], bf16x8 (&fpos)[2], bf16x8 (&fneg)[2]) {
  const float L2E = 1.4426950408889634f;
  float mx[2], nm[2];
#pragma unroll
  for (int t = 0; t < 2; ++t) {
    const f32x4 &p0 = p[t][0], &p1 = p[t][1];
    mx[t] = max3(max3(max3(max3(p0[0], p0[1], p0[2]), p0[3], p1[0]), p1[1], p1[2]), p1[3], p0[0]);
    nm[t] = -min3(min3(min3(min3(p0[0], p0[1], p0[2]), p0[3], p1[0]), p1[1], p1[2]), p1[3], p0[0]);
  }
  xrow4<true>(mx[0], nm[0], mx[1], nm[1]);
  f32x4 e[2][2], n[2][2];
  float sp[2], sn[2];
#pragma unroll
  for (int t = 0; t < 2; ++t) {
    const float a = -mx[t] * L2E, b = -nm[t] * L2E;
#pragma unroll
    for (int h = 0; h < 2; ++h) { e[t][h] = exp4(p[t][h], L2E, a); n[t][h] = exp4(p[t][h], -L2E, b); }
    sp[t] = sum8(e[t][0], e[t][1]);
    sn[t] = sum8(n[t][0], n[t][1]);
  }
  xrow4<false>(sp[0], sn[0], sp[1], sn[1]);
#pragma unroll
  for (int t = 0; t < 2; ++t) {
    const float rp = __builtin_amdgcn_rcpf(sp[t]), rn = __builtin_amdgcn_rcpf(sn[t]);
    fpos[t] = pack8(e[t][0] * rp, e[t][1] * rp);
    fneg[t] = pack8(n[t][0] * rn, n[t][1] * rn);
  }
}

constexpr int K1_A0 = 0, K1_ABUF = 32768, K1_B0 = 65536, K1_BBUF = 40960, K1_WPHI = 147456, K1_DUMP = 147456 + 2048, K1_LDS = 147456 + 2048 + 2048;

constexpr int k1_pos(int i) { return i < 8 ? (i >> 1) + 4 * (i & 1) : 17 - i; }
constexpr unsigned SW = F_ * 2, SQ = 3 * D_ * 2;

struct K1Ctx {
  const char* sA;
  const char* sG; const char* sU; const char* sQ;
  const char* vP2; const char* vP3;
  unsigned stepP3;
  unsigned voffA0, voffA1, voffW, voffQ;
  const char* pf;
  unsigned ldsD;
  const char* vPF;
  unsigned stepPF, pfmask;
  unsigned ldsA, ldsB;
};

template <int BUF> DI void k1_issue_A(K1Ctx& c) {
#pragma unroll
  for (int i = 0; i < 4; ++i) glds_s((i & 1) ? c.voffA1 : c.voffA0, c.sA + i * 16384, c.ldsA + BUF * K1_ABUF + i * 1024);
  c.sA += 128;
  asm volatile("s_mov_b32 m0, %1\n\ts_nop 0\n\tglobal_load_lds_dword %0, off" ::"v"(c.vPF), "s"(c.ldsD) : "memory");
  c.vPF += (c.stepPF & c.pfmask);
}
template <int BUF> DI void k1_issue_B(K1Ctx& c) {
  const unsigned l = c.ldsB + BUF * K1_BBUF;
  glds_s(c.voffW, c.sG, l);             c.sG += 64 * SW;
  glds_s(c.voffW, c.sU, l + 8192);      c.sU += 64 * SW;
  glds_v(c.vP2, l + 2 * 8192);          c.vP2 += 64 * SW;
  glds_v(c.vP3, l + 3 * 8192);          c.vP3 += c.stepP3;
  glds_s(c.voffQ, c.sQ, l + 4 * 8192);  c.sQ += 64 * SQ;
}

template <int BUF, int KS> DI void k1_load(LAS char* lds, const unsigned (&aB)[4], const unsigned (&aA)[2], bf16x8 (&fb)[10], bf16x8 (&fa)[4]) {
#pragma unroll
  for (int m = 0; m < 4; ++m) fa[m] = *(LAS bf16x8*)(lds + aA[KS] + (K1_A0 + BUF * K1_ABUF + m * 2048));
#pragma unroll
  for (int i = 0; i < 10; ++i) fb[i] = tr_frag(lds + aB[k1_pos(i) & 3], (k1_pos(i) >> 2) * 8192 + BUF * K1_BBUF + KS * 4096);
}
DI void k1_mma(f32x4 (&acc)[10][4], const bf16x8 (&fb)[10], const bf16x8 (&fa)[4]) {
  __builtin_amdgcn_s_setprio(1);
#pragma unroll
  for (int i = 0; i < 10; ++i)
#pragma unroll
    for (int m = 0; m < 4; ++m) MFMA_ACC(acc[i][m], fb[i], fa[m]);
  __builtin_amdgcn_s_setprio(0);
}
template <int GRP, int BUF, int MODE>
DI void k1_tile(LAS char* lds, K1Ctx& c, const unsigned (&aB)[4], const unsigned (&aA)[2], f32x4 (&acc)[10][4]) {
  bf16x8 fb[10], fa[4];
  k1_load<BUF, 0>(lds, aB, aA, fb, fa);
  if (MODE < 2) { if (GRP == 0) k1_issue_A<BUF ^ 1>(c); else k1_issue_B<BUF ^ 1>(c); WAIT_V(10); }
  else {
    WAIT_V(0);
    if (c.pf) asm volatile("s_mov_b32 m0, %2\n\ts_nop 0\n\tglobal_load_lds_dword %0, %1" ::"v"(__builtin_amdgcn_mbcnt_hi(~0u, __builtin_amdgcn_mbcnt_lo(~0u, 0u)) * 128u), "s"(c.pf), "s"(c.ldsD) : "memory");
  }
  WAIT_L0();
  BAR();
  k1_mma(acc, fb, fa);
  BAR();
  k1_load<BUF, 1>(lds, aB, aA, fb, fa);
  if (MODE == 0) { if (GRP == 0) k1_issue_B<BUF>(c); else k1_issue_A<BUF>(c); WAIT_V(10); }
  else if (MODE == 1) { WAIT_V(5); }
  WAIT_L0();
  BAR();
  k1_mma(acc, fb, fa);
  if (!(MODE == 2 && GRP == 1)) BAR();
}
template <int GRP>
DI void k1_main(LAS char* lds, K1Ctx& c, const unsigned (&aB)[4], const unsigned (&aA)[2], f32x4 (&acc)[10][4]) {
  if (GRP == 1) BAR();
#pragma unroll 1
  for (int t = 0; t < 14; t += 2) {
    c.pfmask = t <= 10 - GRP ? ~0u : 0u;
    k1_tile<GRP, 0, 0>(lds, c, aB, aA, acc);
    c.pfmask = t + 1 <= 10 - GRP ? ~0u : 0u;
    k1_tile<GRP, 1, 0>(lds, c, aB, aA, acc);
  }
  k1_tile<GRP, 0, 1>(lds, c, aB, aA, acc);
  k1_tile<GRP, 1, 2>(lds, c, aB, aA, acc);
  MFMA_DRAIN();
}

DI void epi_h(const f32x4 (&ga)[4], const f32x4 (&ua)[4], char* pp) {
#pragma unroll
  for (int mp = 0; mp < 2; ++mp) {
    u32x2 w[2];
#pragma unroll
    for (int h = 0; h < 2; ++h) {
      const int m = 2 * mp + h;
      w[h][0] = gelu_mul2(ga[m].xy, ua[m].xy);
      w[h][1] = gelu_mul2(ga[m].zw, ua[m].zw);
    }
    *(u32x4*)(pp + mp * 1024) = widen(w[0], w[1]);
  }
}
DI void epi_q(const f32x4 (&a0)[4], const f32x4 (&a1)[4], char* pp) {
#pragma unroll
  for (int mp = 0; mp < 2; ++mp) {
    *(u32x4*)(pp + mp * 1024) = widen(pk4(a0[2 * mp]), pk4(a0[2 * mp + 1]));
    *(u32x4*)(pp + PANEL + mp * 1024) = widen(pk4(a1[2 * mp]), pk4(a1[2 * mp + 1]));
  }
}
DI void epi_kv(const bf16x8 (&kp)[4], const bf16x8 (&vp)[4], LAS char* wphi  ,
               LAS char* scr  , LAS char* red  , int lane) {
  const int g = lane >> 4, c = lane & 15, q = (lane >> 2) & 3, p = lane & 3;
  bf16x8 wf[2];
#pragma unroll
  for (int fb = 0; fb < 2; ++fb) {
    LAS char* a = wphi + (4 * g + q) * 64 + fb * 32 + p * 8;
    s16x4 lo = __builtin_amdgcn_ds_read_tr16_b64_v4i16((LAS s16x4*)a);
    s16x4 hi = __builtin_amdgcn_ds_read_tr16_b64_v4i16((LAS s16x4*)(a + 1024));
    wf[fb] = __builtin_shufflevector(lo, hi, 0, 1, 2, 3, 4, 5, 6, 7);
  }
  const u32x4 onesu = {0x3F803F80u, 0x3F803F80u, 0x3F803F80u, 0x3F803F80u};
  const bf16x8 ONES = __builtin_bit_cast(bf16x8, onesu);
  const f32x4 Z = {0.f, 0.f, 0.f, 0.f};
  LAS char* wr = scr + c * 200 + g * 8;
  LAS char* rd = scr + (8 * g + q) * 200 + p * 8;
  f32x4 kv[4][3];
#pragma unroll
  for (int a = 0; a < 4; ++a)
#pragma unroll
    for (int b = 0; b < 3; ++b) kv[a][b] = Z;
#pragma unroll
  for (int mp = 0; mp < 2; ++mp) {
    f32x4 pp[2][2];
#pragma unroll
    for (int h = 0; h < 2; ++h) { pp[h][0] = MFMA(wf[0], kp[2 * mp + h], Z); pp[h][1] = MFMA(wf[1], kp[2 * mp + h], Z); }
    bf16x8 fp[2], fn[2];
    feature_map2(pp, fp, fn);
#pragma unroll
    for (int h = 0; h < 2; ++h) {
      const u32x4 a = __builtin_bit_cast(u32x4, fp[h]), b = __builtin_bit_cast(u32x4, fn[h]), v = __builtin_bit_cast(u32x4, vp[2 * mp + h]);
      LAS char* w = wr + h * 3200;
      *(LAS u32x2*)(w) = a.xy;       *(LAS u32x2*)(w + 32) = a.zw;
      *(LAS u32x2*)(w + 64) = b.xy;  *(LAS u32x2*)(w + 96) = b.zw;
      *(LAS u32x2*)(w + 128) = v.xy; *(LAS u32x2*)(w + 160) = v.zw;
    }
    bf16x8 fr[6];
#pragma unroll
    for (int a = 0; a < 6; ++a) {
      s16x4 lo = __builtin_amdgcn_ds_read_tr16_b64_v4i16((LAS s16x4*)(rd + a * 32));
      s16x4 hi = __builtin_amdgcn_ds_read_tr16_b64_v4i16((LAS s16x4*)(rd + a * 32 + 800));
      fr[a] = __builtin_shufflevector(lo, hi, 0, 1, 2, 3, 4, 5, 6, 7);
    }
#pragma unroll
    for (int a = 0; a < 4; ++a) {
      kv[a][0] = MFMA(fr[a], fr[4], kv[a][0]);
      kv[a][1] = MFMA(fr[a], fr[5], kv[a][1]);
      kv[a][2] = MFMA(fr[a], ONES, kv[a][2]);
    }
  }
#pragma unroll
  for (int a = 0; a < 4; ++a) {
    *(LAS f32x4*)(red + (a * 2 + 0) * 1024 + lane * 16) = kv[a][0];
    *(LAS f32x4*)(red + (a * 2 + 1) * 1024 + lane * 16) = kv[a][1];
    if (c == 0) *(LAS f32x4*)(red + 8192 + a * 64 + g * 16) = kv[a][2];
  }
}

__global__ __launch_bounds__(512, 2) void k1_kernel(const unsigned short* __restrict__ x, const unsigned short* __restrict__ wqkv,
                                                    const unsigned short* __restrict__ wphi, const unsigned short* __restrict__ wg,
                                                    const unsigned short* __restrict__ wu, const unsigned short* __restrict__ gatew,
                                                    char* __restrict__ act, char* __restrict__ qbuf,
                                                    float* __restrict__ kvp, float* __restrict__ gate, unsigned* __restrict__ flags,
                                                    const char* __restrict__ wd, const char* __restrict__ wo) {
  extern __shared__ __attribute__((aligned(16))) char smem[];
  LAS char* lds = (LAS char*)smem;
  const unsigned ldsb = (unsigned)(size_t)lds;
  const int tid = threadIdx.x, lane = tid & 63;
  const int wid = __builtin_amdgcn_readfirstlane(tid >> 6);
  const int grp = wid >> 2, j = wid & 3, wm = 2 * grp + (j >> 1), wn = (j & 1) ^ grp;
  const int bid = blockIdx.x, xcd = bid & 7, idx = bid >> 3;
  const int rt = idx & 7, cg = xcd * 4 + (idx >> 3);
  const int row0 = rt * 256;
  const int g = lane >> 4, c = lane & 15;

  STAMP_DECL;
  u32x4 gx0, gx1, gw0, gw1;
  {
    const char* xr = (const char*)x + (size_t)(bid * 8 + wid) * 2048;
    const unsigned vo = lane * 16;
    asm volatile("global_load_dwordx4 %0, %4, %5\n\tglobal_load_dwordx4 %1, %4, %5 offset:1024\n\t"
                 "global_load_dwordx4 %2, %4, %6\n\tglobal_load_dwordx4 %3, %4, %6 offset:1024"
                 : "=&v"(gx0), "=&v"(gx1), "=&v"(gw0), "=&v"(gw1) : "v"(vo), "s"(xr), "s"(gatew) : "memory");
  }

  K1Ctx cx;
  {
    const int ar = (grp == 0 ? 128 : 0) + 32 * j;
    cx.sA = (const char*)x + (unsigned)(row0 + ar) * 2048u;
    cx.ldsA = ldsb + K1_A0 + ar * 128;
    const int r = lane >> 3, ch = lane & 7;
    cx.voffA0 = r * 2048 + ((ch ^ (r >> 1)) * 16);
    cx.voffA1 = cx.voffA0 ^ 64;
    const unsigned kr = grp * 32 + 8 * j;
    const int l32 = (ch >> 1) ^ (((r >> 1) & 1) | ((j & 1) << 1));
    const unsigned h16 = (ch & 1) * 16;
    cx.sG = (const char*)wg + kr * SW + cg * 224;
    cx.sU = (const char*)wu + kr * SW + cg * 224;
    cx.sQ = (const char*)wqkv + kr * SQ + cg * 64;
    cx.voffW = r * SW + l32 * 32 + h16;
    cx.voffQ = r * SQ + (l32 < 2 ? 4096 + l32 * 32 : (l32 - 2) * 32) + h16;
    cx.vP2 = (l32 ? cx.sG + 96 + l32 * 32 : cx.sU + 128) + (r * SW + h16);
    cx.vP3 = l32 < 2 ? cx.sU + (160 + l32 * 32 + r * SW + h16) : cx.sQ + (2048 + (l32 - 2) * 32 + r * SQ + h16);
    cx.stepP3 = l32 < 2 ? 64 * SW : 64 * SQ;
    cx.ldsB = ldsb + K1_B0 + kr * 128;
    const int id = wid * 256 + bid;
    cx.pf = id < 896 ? wd + (size_t)id * 8192 : (id < 1152 ? wo + (size_t)(id - 896) * 8192 : nullptr);
    cx.ldsD = ldsb + K1_DUMP + wid * 256;
    {
      const unsigned e = wid * 13 + (lane < 12 ? lane : 12);
      const unsigned k2 = e >= 20 ? 1u : 0u, ln = e - 20 * k2, krow = 192 + idx * 2 + k2;
      cx.vPF = e >= 40 ? (const char*)x + ((idx * 64 + e - 40) * 2048u + 384u)
             : ln < 7  ? (const char*)wg + (krow * SW + xcd * 896 + ln * 128)
             : ln < 14 ? (const char*)wu + (krow * SW + xcd * 896 + (ln - 7) * 128)
                       : (const char*)wqkv + (krow * SQ + xcd * 256 + ((ln - 14) >> 1) * 2048 + ((ln - 14) & 1) * 128);
      cx.stepPF = e >= 40 ? 128u : (ln < 14 ? 64 * SW : 64 * SQ);
      cx.pfmask = ~0u;
    }
    cx.sA = uni_ptr(cx.sA); cx.sG = uni_ptr(cx.sG); cx.sU = uni_ptr(cx.sU); cx.sQ = uni_ptr(cx.sQ);
    asm volatile("s_nop 4" : "+s"(cx.sA), "+s"(cx.sG), "+s"(cx.sU), "+s"(cx.sQ));
  }
  if (wid < 2) glds_s(lane * 16, (const char*)wphi + cg * 2048 + wid * 1024, ldsb + K1_WPHI + wid * 1024);
  if (grp == 0) { k1_issue_B<0>(cx); k1_issue_A<0>(cx); k1_issue_B<1>(cx); }
  else          { k1_issue_A<0>(cx); k1_issue_B<0>(cx); k1_issue_A<1>(cx); }

  unsigned aB[4], aA[2];
  {
    const int q = (lane >> 2) & 3, p = lane & 3;
    const int swz = ((q >> 1) & 1) | ((g & 1) << 1);
    const unsigned lb = K1_B0 + (8 * g + q) * 128 + p * 8;
#pragma unroll
    for (int i = 0; i < 4; ++i) aB[i] = lb + ((i ^ swz ^ (2 * wn)) * 32) + wn * (2 + (i >> 1)) * 8192;
    const int sa = (c >> 1) & 7;
    aA[0] = (64 * wm + c) * 128 + ((g ^ sa) * 16);
    aA[1] = aA[0] ^ 64;
  }

  {
    asm volatile("s_waitcnt vmcnt(15)" : "+v"(gx0), "+v"(gx1), "+v"(gw0), "+v"(gw1)::"memory");
    float s = 0.f;
#pragma unroll
    for (int e = 0; e < 4; ++e) {
      s = __builtin_fmaf(__uint_as_float(gx0[e] << 16), __uint_as_float(gw0[e] << 16), s);
      s = __builtin_fmaf(__uint_as_float(gx0[e] & 0xffff0000u), __uint_as_float(gw0[e] & 0xffff0000u), s);
      s = __builtin_fmaf(__uint_as_float(gx1[e] << 16), __uint_as_float(gw1[e] << 16), s);
      s = __builtin_fmaf(__uint_as_float(gx1[e] & 0xffff0000u), __uint_as_float(gw1[e] & 0xffff0000u), s);
    }
#pragma unroll
    for (int o = 1; o < 64; o <<= 1) s += __shfl_xor(s, o);
    const float gv = 1.0f / (1.0f + __expf(-s));
    if (lane == 0) {
      float* gp = gate + bid * 8 + wid;
      asm volatile("global_store_dword %0, %1, %2" ::"v"(0u), "v"(gv), "s"(gp) : "memory");
    }
  }

  f32x4 acc[10][4];
#pragma unroll
  for (int i = 0; i < 10; ++i)
#pragma unroll
    for (int m = 0; m < 4; ++m) acc[i][m] = (f32x4){0.f, 0.f, 0.f, 0.f};

  WAIT_V(10);
  BAR();
  STAMP(1);
  if (grp == 0) k1_main<0>(lds, cx, aB, aA, acc); else k1_main<1>(lds, cx, aB, aA, acc);

  STAMP(2);
  int tz = threadIdx.x;
  asm volatile("" : "+v"(tz));
  const int lane2 = tz & 63, g2 = lane2 >> 4;
  if (tz < 8) flags[(16 + bid * 8 + tz) * 16] = 0u;
  if (bid == 0 && tz >= 64 && tz < 72) flags[(tz - 64) * 16] = 0u;
  const unsigned lo = (unsigned)(row0 + 64 * wm + 16 * (g2 & 1) + (lane2 & 15)) * 32u + (g2 >> 1) * 16u;
  char* hp = act + (size_t)(cg * 7 + 5 * wn) * PANEL + lo;
  if (wn == 0) {
#pragma unroll
    for (int sl = 0; sl < 5; ++sl) epi_h(acc[2 * sl], acc[2 * sl + 1], hp + (size_t)sl * PANEL);
  } else {
    epi_q(acc[9], acc[8], qbuf + (size_t)(2 * cg) * PANEL + lo);
    epi_h(acc[0], acc[4], hp);
    epi_h(acc[2], acc[6], hp + PANEL);
    STAMP(3);
    bf16x8 kp[4], vp[4];
#pragma unroll
    for (int m = 0; m < 4; ++m) { kp[m] = pack8(acc[1][m], acc[3][m]); vp[m] = pack8(acc[5][m], acc[7][m]); }
    epi_kv(kp, vp, lds + K1_WPHI, lds + 4 * 8448 + wm * 6400, lds + wm * 8448, lane2);
  }
  STAMP(4);
  __syncthreads();
  STAMP(5);
  for (int e = tz; e < 528; e += 512) {
    LAS char* p = lds + e * 16;
    const f32x4 s = (*(LAS f32x4*)p + *(LAS f32x4*)(p + 8448)) + (*(LAS f32x4*)(p + 2 * 8448) + *(LAS f32x4*)(p + 3 * 8448));
    *(f32x4*)(kvp + ((size_t)rt * 32 + cg) * KVP + e * 4) = s;
  }
  STAMP(6);
  STAMP_OUT((char*)gate + (1u << 20), wn);
}

constexpr int K2_ST = 32768, K2_NST = 4, K2_AUX = K2_ST * K2_NST, K2_LDS = K2_AUX + 8192;
constexpr int FL_OCNT = 0;
constexpr int FL_X = 16;
#define RLX_AGENT __ATOMIC_RELAXED, __HIP_MEMORY_SCOPE_AGENT

DI void glds_s1(unsigned voff, const void* sbase, unsigned ldsaddr) {
  asm volatile("s_mov_b32 m0, %2\n\ts_nop 0\n\tglobal_load_lds_dwordx4 %0, %1 sc1" ::"v"(voff), "s"(sbase), "s"(ldsaddr) : "memory");
}
DI void st16_sc1(void* p, u32x4 v) { asm volatile("global_store_dwordx4 %0, %1, off sc1\n\ts_nop 1" ::"v"(p), "v"(v) : "memory"); }
DI void wait_ge(const unsigned* p, unsigned need) {
  for (unsigned spins = 0; spins < (1u << 22); ++spins) {
    if ((unsigned)__builtin_amdgcn_readfirstlane(__hip_atomic_load(p, RLX_AGENT)) >= need) break;
    __builtin_amdgcn_s_sleep(1);
  }
}

DI f32x4 sel4(int cnd, f32x4 a, f32x4 b) { return cnd ? a : b; }

struct K2Ctx {
  const char* sA; const char* sB;
  unsigned voffB, vlin;
  unsigned ldsA, ldsB;
};
template <bool SC1> DI void k2_issue(K2Ctx& c, int slot) {
  const unsigned o = slot * K2_ST;
  if (SC1) { glds_s1(c.vlin, c.sA, c.ldsA + o); glds_s1(c.vlin, c.sA + 1024, c.ldsA + o + 1024); }
  else     { glds_s(c.vlin, c.sA, c.ldsA + o); glds_s(c.vlin, c.sA + 1024, c.ldsA + o + 1024); }
  glds_s(c.voffB, c.sB, c.ldsB + o);
  glds_s(c.voffB, c.sB + 128, c.ldsB + o + 8192);
  c.sA += 4 * PANEL;
  c.sB += 64 * 2048;
}
template <int SLOT> DI void k2_load(LAS char* lds, unsigned aA, const unsigned (&aB)[4], bf16x8 (&fb)[4], bf16x8 (&fa)[4]) {
#pragma unroll
  for (int m = 0; m < 4; ++m) fa[m] = *(LAS bf16x8*)(lds + aA + ((SLOT & 1) * K2_ST + m * 512));
#pragma unroll
  for (int i = 0; i < 4; ++i) fb[i] = tr_frag(lds + aB[i], (SLOT & 1) * K2_ST);
}
DI void k2_mma(f32x4 (&acc)[4][4], const bf16x8 (&fb)[4], const bf16x8 (&fa)[4]) {
  __builtin_amdgcn_s_setprio(1);
#pragma unroll
  for (int i = 0; i < 4; ++i)
#pragma unroll
    for (int m = 0; m < 4; ++m) MFMA_ACC(acc[i][m], fb[i], fa[m]);
  __builtin_amdgcn_s_setprio(0);
}
template <int SLOT, int ISSUE, int WAITN, bool LASTBAR>
DI void k2_step(LAS char* lds, K2Ctx& c, const unsigned (&aA)[2], const unsigned (&aB)[2][4], f32x4 (&acc)[4][4]) {
  bf16x8 fb[4], fa[4];
  k2_load<SLOT>(lds, aA[SLOT >> 1], aB[SLOT >> 1], fb, fa);
  if (ISSUE == 1) k2_issue<false>(c, (SLOT + 3) & 3);
  if (ISSUE == 2) k2_issue<true>(c, (SLOT + 3) & 3);
  asm volatile("s_waitcnt vmcnt(%0)" ::"i"(WAITN) : "memory");
  WAIT_L0();
  BAR();
  k2_mma(acc, fb, fa);
  if (LASTBAR) BAR();
}
template <int ISSUE> DI void k2_step4(LAS char* lds, K2Ctx& c, const unsigned (&aA)[2], const unsigned (&aB)[2][4], f32x4 (&acc)[4][4]) {
  k2_step<0, ISSUE, 8, true>(lds, c, aA, aB, acc);
  k2_step<1, ISSUE, 8, true>(lds, c, aA, aB, acc);
  k2_step<2, ISSUE, 8, true>(lds, c, aA, aB, acc);
  k2_step<3, ISSUE, 8, true>(lds, c, aA, aB, acc);
}

__global__ __launch_bounds__(512, 2) void k2_kernel(char* __restrict__ act, const char* __restrict__ qbuf,
                                                    const unsigned short* __restrict__ wphi, const char* __restrict__ wd,
                                                    const char* __restrict__ wo, const float* __restrict__ kvp,
                                                    const float* __restrict__ gate, char* __restrict__ xbuf,
                                                    unsigned* __restrict__ flags, unsigned short* __restrict__ out) {
  extern __shared__ __attribute__((aligned(16))) char smem[];
  LAS char* lds = (LAS char*)smem;
  const unsigned ldsb = (unsigned)(size_t)lds;
  const int tid = threadIdx.x, lane = tid & 63;
  const int wid = __builtin_amdgcn_readfirstlane(tid >> 6);
  const int grp = wid >> 2, j = wid & 3, wm = j >> 1, wn = j & 1;
  const int bid = blockIdx.x, xcd = bid & 7, idx = bid >> 3;
  const int ks = xcd >> 2, qd = xcd & 3;
  const int tm = (qd >> 1) * 8 + (idx & 7), tn = (qd & 1) * 4 + (idx >> 3);
  const int g = lane >> 4, c = lane & 15, q = (lane >> 2) & 3, p = lane & 3;

  STAMP_DECL;
  K2Ctx cx;
  {
    const int r = lane >> 3, ch = lane & 7;
    const int kb = 2 * grp + (j >> 1);
    cx.sA = act + (size_t)(ks * 144 + kb) * PANEL + (tm * 128 + (j & 1) * 64) * 32;
    cx.ldsA = ldsb + kb * 4096 + (j & 1) * 2048;
    cx.vlin = lane * 16;
    const int kr = 32 * grp + 8 * j;
    cx.sB = wd + (size_t)(ks * 2304 + kr) * 2048 + tn * 256;
    cx.voffB = r * 2048 + (((ch >> 1) ^ (((r >> 1) & 1) | ((j & 1) << 1))) * 32) + (ch & 1) * 16;
    cx.ldsB = ldsb + 16384 + kr * 128;
  }
  const int head = bid & 31, rb = bid >> 5;
  float og[4];
  {
    LAS char* aux = lds + K2_AUX;
    const char* kvb = (const char*)kvp + (size_t)head * (KVP * 4);
    constexpr unsigned RS = 32u * KVP * 4u;
    f32x4 zq[8], kq[8];
    if (wid == 0) {
      const unsigned vo = (512 + c) * 16;
      asm volatile("global_load_dwordx4 %0, %8, %9\n\tglobal_load_dwordx4 %1, %8, %10\n\tglobal_load_dwordx4 %2, %8, %11\n\tglobal_load_dwordx4 %3, %8, %12\n\t"
                   "global_load_dwordx4 %4, %8, %13\n\tglobal_load_dwordx4 %5, %8, %14\n\tglobal_load_dwordx4 %6, %8, %15\n\tglobal_load_dwordx4 %7, %8, %16"
                   : "=&v"(zq[0]), "=&v"(zq[1]), "=&v"(zq[2]), "=&v"(zq[3]), "=&v"(zq[4]), "=&v"(zq[5]), "=&v"(zq[6]), "=&v"(zq[7])
                   : "v"(vo), "s"(kvb), "s"(kvb + RS), "s"(kvb + 2 * RS), "s"(kvb + 3 * RS), "s"(kvb + 4 * RS), "s"(kvb + 5 * RS), "s"(kvb + 6 * RS), "s"(kvb + 7 * RS)
                   : "memory");
    } else if (wid <= 2) {
      glds_s(lane * 16, (const char*)wphi + (size_t)head * 2048 + (wid - 1) * 1024, ldsb + K2_AUX + 528 * 8 + (wid - 1) * 1024);
    }
    {
      const unsigned vo = tid * 16;
      asm volatile("global_load_dwordx4 %0, %8, %9\n\tglobal_load_dwordx4 %1, %8, %10\n\tglobal_load_dwordx4 %2, %8, %11\n\tglobal_load_dwordx4 %3, %8, %12\n\t"
                   "global_load_dwordx4 %4, %8, %13\n\tglobal_load_dwordx4 %5, %8, %14\n\tglobal_load_dwordx4 %6, %8, %15\n\tglobal_load_dwordx4 %7, %8, %16"
                   : "=&v"(kq[0]), "=&v"(kq[1]), "=&v"(kq[2]), "=&v"(kq[3]), "=&v"(kq[4]), "=&v"(kq[5]), "=&v"(kq[6]), "=&v"(kq[7])
                   : "v"(vo), "s"(kvb), "s"(kvb + RS), "s"(kvb + 2 * RS), "s"(kvb + 3 * RS), "s"(kvb + 4 * RS), "s"(kvb + 5 * RS), "s"(kvb + 6 * RS), "s"(kvb + 7 * RS)
                   : "memory");
    }
    bf16x8 qf[2];
    float gt[2];
    {
      const char* qb = qbuf + (size_t)(2 * head) * PANEL + (rb * 256 + wid * 32) * 32;
      const unsigned vq = (g >> 1) * PANEL + c * 32 + (g & 1) * 16, vg = c * 4;
      const float* g0 = gate + rb * 256 + wid * 32;
      const float* g1 = gate + tm * 128 + 64 * wm;
      asm volatile("global_load_dwordx4 %0, %8, %10\n\tglobal_load_dwordx4 %1, %8, %10 offset:512\n\t"
                   "global_load_dword %2, %9, %11\n\tglobal_load_dword %3, %9, %11 offset:64\n\t"
                   "global_load_dword %4, %9, %12\n\tglobal_load_dword %5, %9, %12 offset:64\n\t"
                   "global_load_dword %6, %9, %12 offset:128\n\tglobal_load_dword %7, %9, %12 offset:192"
                   : "=&v"(qf[0]), "=&v"(qf[1]), "=&v"(gt[0]), "=&v"(gt[1]), "=&v"(og[0]), "=&v"(og[1]), "=&v"(og[2]), "=&v"(og[3])
                   : "v"(vq), "v"(vg), "s"(qb), "s"(g0), "s"(g1) : "memory");
    }
    k2_issue<false>(cx, 0); k2_issue<false>(cx, 1); k2_issue<false>(cx, 2);
    asm volatile("s_waitcnt vmcnt(20)" : "+v"(kq[0]), "+v"(kq[1]), "+v"(kq[2]), "+v"(kq[3]), "+v"(kq[4]), "+v"(kq[5]), "+v"(kq[6]), "+v"(kq[7])::"memory");
    *(LAS u32x2*)(aux + tid * 8) = pk4(((kq[0] + kq[1]) + (kq[2] + kq[3])) + ((kq[4] + kq[5]) + (kq[6] + kq[7])));
    if (wid == 0) {
      asm volatile("" : "+v"(zq[0]), "+v"(zq[1]), "+v"(zq[2]), "+v"(zq[3]), "+v"(zq[4]), "+v"(zq[5]), "+v"(zq[6]), "+v"(zq[7]));
      *(LAS u32x2*)(aux + (512 + c) * 8) = pk4(((zq[0] + zq[1]) + (zq[2] + zq[3])) + ((zq[4] + zq[5]) + (zq[6] + zq[7])));
    }
    asm volatile("s_waitcnt vmcnt(12)" : "+v"(qf[0]), "+v"(qf[1]), "+v"(gt[0]), "+v"(gt[1]), "+v"(og[0]), "+v"(og[1]), "+v"(og[2]), "+v"(og[3])::"memory");
    WAIT_L0();
    BAR();
    bf16x8 wf[2];
#pragma unroll
    for (int fb = 0; fb < 2; ++fb) {
      LAS char* a = aux + 528 * 8 + (8 * g + q) * 64 + fb * 32 + p * 8;
      s16x4 lo = __builtin_amdgcn_ds_read_tr16_b64_v4i16((LAS s16x4*)a);
      s16x4 hi = __builtin_amdgcn_ds_read_tr16_b64_v4i16((LAS s16x4*)(a + 256));
      wf[fb] = __builtin_shufflevector(lo, hi, 0, 1, 2, 3, 4, 5, 6, 7);
    }
    bf16x8 kf[2][2], zf[2];
#pragma unroll
    for (int h = 0; h < 2; ++h) {
#pragma unroll
      for (int db = 0; db < 2; ++db) {
        const u32x2 lo = *(LAS u32x2*)(aux + (((2 * h) * 2 + db) * 64 + lane) * 8);
        const u32x2 hi = *(LAS u32x2*)(aux + (((2 * h + 1) * 2 + db) * 64 + lane) * 8);
        const u32x4 w = {lo[0], lo[1], hi[0], hi[1]};
        kf[h][db] = __builtin_bit_cast(bf16x8, w);
      }
      const u32x2 lo = *(LAS u32x2*)(aux + (512 + (2 * h) * 4 + g) * 8);
      const u32x2 hi = *(LAS u32x2*)(aux + (512 + (2 * h + 1) * 4 + g) * 8);
      const u32x4 w = {lo[0], lo[1], hi[0], hi[1]};
      zf[h] = __builtin_bit_cast(bf16x8, w);
    }
    const f32x4 Z = {0.f, 0.f, 0.f, 0.f};
    f32x4 pp[2][2];
#pragma unroll
    for (int m = 0; m < 2; ++m) { pp[m][0] = MFMA(wf[0], qf[m], Z); pp[m][1] = MFMA(wf[1], qf[m], Z); }
    bf16x8 fp[2], fn[2];
    feature_map2(pp, fp, fn);
    u32x2 w0[2], w1[2];
#pragma unroll
    for (int m = 0; m < 2; ++m) {
      f32x4 n0 = MFMA(kf[0][0], fp[m], Z); n0 = MFMA(kf[1][0], fn[m], n0);
      f32x4 n1 = MFMA(kf[0][1], fp[m], Z); n1 = MFMA(kf[1][1], fn[m], n1);
      f32x4 dn = MFMA(zf[0], fp[m], Z); dn = MFMA(zf[1], fn[m], dn);
      const float sc = gt[m] / (dn[0] + 1e-6f);
      w0[m] = pk4(n0 * sc); w1[m] = pk4(n1 * sc);
    }
    char* op = act + (size_t)(224 + 2 * head) * PANEL + (rb * 256 + wid * 32 + 16 * (g & 1) + c) * 32 + (g >> 1) * 16;
    st16_sc1(op, widen(w0[0], w0[1]));
    st16_sc1(op + PANEL, widen(w1[0], w1[1]));
  }

  STAMP(1);
  unsigned aA[2], aB[2][4];
  {
    aA[0] = (2 * grp + (g >> 1)) * 4096 + (64 * wm + c) * 32 + (g & 1) * 16;
    aA[1] = aA[0] + 2 * K2_ST;
    const int swz = ((q >> 1) & 1) | ((g & 1) << 1);
#pragma unroll
    for (int i = 0; i < 4; ++i) {
      aB[0][i] = 16384 + wn * 8192 + (32 * grp + 8 * g + q) * 128 + p * 8 + ((i ^ swz) * 32);
      aB[1][i] = aB[0][i] + 2 * K2_ST;
    }
  }
#pragma unroll
  for (int m = 0; m < 4; ++m) og[m] = 1.0f - og[m];
  f32x4 acc[4][4];
#pragma unroll
  for (int i = 0; i < 4; ++i)
#pragma unroll
    for (int m = 0; m < 4; ++m) acc[i][m] = (f32x4){0.f, 0.f, 0.f, 0.f};

  WAIT_V(10);
  BAR();
  if (grp == 1) BAR();
#pragma unroll 1
  for (int t = 0; t < 16; t += 4) {
    if (t == 4 && tid == 0) __hip_atomic_fetch_add(flags + (FL_OCNT + rb) * 16, 1u, RLX_AGENT);
    k2_step4<1>(lds, cx, aA, aB, acc);
  }
  k2_step<0, 1, 8, false>(lds, cx, aA, aB, acc);
  STAMP(2);
  if (ks == 1) {
    if (wid == 0) {
      wait_ge(flags + (FL_OCNT + (tm >> 1)) * 16, 32u);
    }
    cx.sB = wo + (32 * grp + 8 * j) * 2048 + tn * 256;
  }
  BAR();
  STAMP(3);
  k2_step<1, 2, 8, true>(lds, cx, aA, aB, acc);
  k2_step<2, 2, 8, true>(lds, cx, aA, aB, acc);
  k2_step<3, 2, 8, true>(lds, cx, aA, aB, acc);
  if (ks == 1) {
    MFMA_DRAIN();
#pragma unroll
    for (int i = 0; i < 4; ++i)
#pragma unroll
      for (int m = 0; m < 4; ++m) acc[i][m] *= og[m];
    SCHED(); asm volatile("s_nop 1"); SCHED();
  }
#pragma unroll 1
  for (int t = 20; t < 32; t += 4) k2_step4<2>(lds, cx, aA, aB, acc);
  k2_step<0, 2, 8, true>(lds, cx, aA, aB, acc);
  k2_step<1, 0, 4, true>(lds, cx, aA, aB, acc);
  k2_step<2, 0, 0, true>(lds, cx, aA, aB, acc);
  k2_step<3, 0, 0, false>(lds, cx, aA, aB, acc);
  STAMP(4);
  if (grp == 0) BAR();
  MFMA_DRAIN();
  if (ks == 0) {
#pragma unroll
    for (int i = 0; i < 4; ++i)
#pragma unroll
      for (int m = 0; m < 4; ++m) acc[i][m] *= og[m];
  }

  f32x4 mine[2][4];
  {
    LAS char* wr = lds + wid * 8192 + lane * 16;
    LAS char* rd = lds + (wid ^ 4) * 8192 + lane * 16;
#pragma unroll
    for (int i = 0; i < 2; ++i)
#pragma unroll
      for (int m = 0; m < 4; ++m) {
        *(LAS f32x4*)(wr + (i * 4 + m) * 1024) = sel4(grp, acc[i][m], acc[2 + i][m]);
        mine[i][m] = sel4(grp, acc[2 + i][m], acc[i][m]);
      }
    __syncthreads();
#pragma unroll
    for (int i = 0; i < 2; ++i)
#pragma unroll
      for (int m = 0; m < 4; ++m) mine[i][m] += *(LAS f32x4*)(rd + (i * 4 + m) * 1024);
  }
  STAMP(5);
  {
    char* xs = xbuf + ((size_t)bid * 8 + wid) * 4096 + lane * 16;
#pragma unroll
    for (int i = 0; i < 2; ++i)
#pragma unroll
      for (int m = 0; m < 2; ++m)
        st16_sc1(xs + (i * 2 + m) * 1024, __builtin_bit_cast(u32x4, sel4(ks, mine[i][m], mine[i][2 + m])));
    WAIT_V(0);
    if (lane == 0) __hip_atomic_store(flags + (FL_X + bid * 8 + wid) * 16, 1u, RLX_AGENT);
    wait_ge(flags + (FL_X + (bid ^ 4) * 8 + wid) * 16, 1u);
    __builtin_amdgcn_fence(__ATOMIC_ACQUIRE, "wavefront");
    STAMP(6);
    const char* xr = xbuf + ((size_t)(bid ^ 4) * 8 + wid) * 4096 + lane * 16;
    f32x4 o[4];
    asm volatile("global_load_dwordx4 %0, %4, off sc1\n\tglobal_load_dwordx4 %1, %4, off offset:1024 sc1\n\t"
                 "global_load_dwordx4 %2, %4, off offset:2048 sc1\n\tglobal_load_dwordx4 %3, %4, off offset:3072 sc1\n\t"
                 "s_waitcnt vmcnt(0)"
                 : "=&v"(o[0]), "=&v"(o[1]), "=&v"(o[2]), "=&v"(o[3]) : "v"(xr) : "memory");
#pragma unroll
    for (int m = 0; m < 2; ++m) {
      const f32x4 v0 = sel4(ks, mine[0][2 + m], mine[0][m]) + o[m];
      const f32x4 v1 = sel4(ks, mine[1][2 + m], mine[1][m]) + o[2 + m];
      const u32x4 w = widen(pk4(v0), pk4(v1));
      unsigned short* op = out + (size_t)(tm * 128 + 64 * wm + 32 * ks + 16 * m + c) * D_ + tn * 128 + 64 * wn + 32 * grp + 16 * (g & 1) + 8 * (g >> 1);
      *(u32x4*)op = w;
    }
  }
  STAMP_OUT((char*)gate + (2u << 20), ks);
}

extern "C" void kernel_launch(void* const* d_in, const int* in_sizes, int n_in, void* d_out, int out_size, void* d_ws,
                              size_t ws_size, hipStream_t stream) {
  const unsigned short* x = (const unsigned short*)d_in[0];
  const unsigned short* wqkv = (const unsigned short*)d_in[1];
  const unsigned short* wphi = (const unsigned short*)d_in[2];
  const char* wo = (const char*)d_in[3];
  const unsigned short* wg = (const unsigned short*)d_in[5];
  const unsigned short* wu = (const unsigned short*)d_in[6];
  const char* wd = (const char*)d_in[7];
  const unsigned short* gatew = (const unsigned short*)d_in[8];
  char* ws = (char*)d_ws;
  char* act = ws;
  char* qbuf = ws + (18u << 20);
  float* kvp = (float*)(ws + (22u << 20));
  char* xbuf = ws + (25u << 20) ;
  unsigned* flags = (unsigned*)(ws + (33u << 20));
  float* gate = (float*)(ws + (34u << 20));
  (void)hipFuncSetAttribute((const void*)k1_kernel, hipFuncAttributeMaxDynamicSharedMemorySize, K1_LDS);
  (void)hipFuncSetAttribute((const void*)k2_kernel, hipFuncAttributeMaxDynamicSharedMemorySize, K2_LDS);
  k1_kernel<<<256, 512, K1_LDS, stream>>>(x, wqkv, wphi, wg, wu, gatew, act, qbuf, kvp, gate, flags, wd, wo);
  k2_kernel<<<256, 512, K2_LDS, stream>>>(act, qbuf, wphi, wd, wo, kvp, gate, xbuf, flags, (unsigned short*)d_out);
}
```

```cpp
#include <hip/hip_runtime.h>
#include <hip/hip_bf16.h>
#include <stdint.h>

typedef short bf16x8 __attribute__((ext_vector_type(8)));
typedef short s16x4 __attribute__((ext_vector_type(4)));
typedef float f32x4 __attribute__((ext_vector_type(4)));
typedef float f32x2 __attribute__((ext_vector_type(2)));
typedef unsigned u32x4 __attribute__((ext_vector_type(4)));
typedef unsigned u32x2 __attribute__((ext_vector_type(2)));
typedef __bf16 bf16x2_t __attribute__((ext_vector_type(2)));
#define LAS __attribute__((address_space(3)))
#define GAS __attribute__((address_space(1)))
#define DI __device__ __forceinline__

constexpr int S_ = 2048, D_ = 1024, F_ = 3584;
constexpr int PANEL = S_ * 32;
constexpr int KVP = 2112;

#define STAMP_DECL
#define STAMP(i)
#define STAMP_OUT(base, tag)
DI void glds_s(unsigned voff, const void* sbase, unsigned ldsaddr) {
  asm volatile("s_mov_b32 m0, %2\n\ts_nop 0\n\tglobal_load_lds_dwordx4 %0, %1" ::"v"(voff), "s"(sbase), "s"(ldsaddr) : "memory");
}
DI void glds_v(const void* vaddr, unsigned ldsaddr) {
  asm volatile("s_mov_b32 m0, %1\n\ts_nop 0\n\tglobal_load_lds_dwordx4 %0, off" ::"v"(vaddr), "s"(ldsaddr) : "memory");
}
DI const char* uni_ptr(const char* p) {
  const unsigned long long v = (unsigned long long)p;
  const unsigned lo = __builtin_amdgcn_readfirstlane((unsigned)v), hi = __builtin_amdgcn_readfirstlane((unsigned)(v >> 32));
  return (const char*)(((unsigned long long)hi << 32) | lo);
}
DI void padd(const char*& p, unsigned step) {
  unsigned lo = (unsigned)(unsigned long long)p, hi = (unsigned)((unsigned long long)p >> 32);
  asm volatile("v_add_co_u32 %0, vcc, %0, %2\n\tv_addc_co_u32 %1, vcc, 0, %1, vcc" : "+v"(lo), "+v"(hi) : "v"(step) : "vcc");
  p = (const char*)(((unsigned long long)hi << 32) | lo);
}
#define WAIT_V(n) asm volatile("s_waitcnt vmcnt(" #n ")" ::: "memory")
#define WAIT_L0() __builtin_amdgcn_s_waitcnt(0xC07F)
#define SCHED() __builtin_amdgcn_sched_barrier(0)
#define BAR() do { SCHED(); asm volatile("" ::: "memory"); __builtin_amdgcn_s_barrier(); asm volatile("" ::: "memory"); SCHED(); } while (0)
#define MFMA_ACC(c, a, b) asm volatile("v_mfma_f32_16x16x32_bf16 %0, %1, %2, %0" : "+v"(c) : "v"(a), "v"(b))
#define MFMA_ACC0(c, a, b) asm volatile("v_mfma_f32_16x16x32_bf16 %0, %1, %2, 0" : "=v"(c) : "v"(a), "v"(b))
#define MFMA_DRAIN() do { SCHED(); asm volatile("s_nop 7\n\ts_nop 7" ::: "memory"); SCHED(); } while (0)
#define MFMA(a, b, c) __builtin_amdgcn_mfma_f32_16x16x32_bf16((a), (b), (c), 0, 0, 0)

DI unsigned pk2(float lo, float hi) {
  f32x2 v = {lo, hi};
  bf16x2_t r = __builtin_convertvector(v, bf16x2_t);
  return __builtin_bit_cast(unsigned, r);
}
DI bf16x8 pack8(const f32x4& a, const f32x4& b) {
  u32x4 p = {pk2(a[0], a[1]), pk2(a[2], a[3]), pk2(b[0], b[1]), pk2(b[2], b[3])};
  return __builtin_bit_cast(bf16x8, p);
}
DI bf16x8 tr_frag(LAS char* p, int off) {
  s16x4 a = __builtin_amdgcn_ds_read_tr16_b64_v4i16((LAS s16x4*)(p + off));
  s16x4 b = __builtin_amdgcn_ds_read_tr16_b64_v4i16((LAS s16x4*)(p + off + 512));
  return __builtin_shufflevector(a, b, 0, 1, 2, 3, 4, 5, 6, 7);
}
DI float bf2f(unsigned short u) { return __uint_as_float(((unsigned)u) << 16); }
DI unsigned gelu_mul2(f32x2 x, f32x2 u) {
  const float c0 = -2.3022081984f;
  const float c1 = -2.3022081984f * 0.044715f;
  const f32x2 p = __builtin_elementwise_fma(x * x, (f32x2){c1, c1}, (f32x2){c0, c0});
  const f32x2 a = x * p;
  const f32x2 e = {__builtin_amdgcn_exp2f(a[0]), __builtin_amdgcn_exp2f(a[1])};
  const f32x2 d = e + 1.0f;
  const f32x2 r = {__builtin_amdgcn_rcpf(d[0]), __builtin_amdgcn_rcpf(d[1])};
  const f32x2 o = (x * u) * r;
  return pk2(o[0], o[1]);
}
DI void swap16(float& a, float& b) {
  auto r = __builtin_amdgcn_permlane16_swap(__float_as_uint(a), __float_as_uint(b), false, false);
  a = __uint_as_float(r[0]); b = __uint_as_float(r[1]);
}
DI void swap32(float& a, float& b) {
  auto r = __builtin_amdgcn_permlane32_swap(__float_as_uint(a), __float_as_uint(b), false, false);
  a = __uint_as_float(r[0]); b = __uint_as_float(r[1]);
}
template <bool MAX> DI float rop(float a, float b) { return MAX ? fmaxf(a, b) : a + b; }
template <bool MAX> DI void xrow4(float& a, float& b, float& c, float& d) {
  swap32(a, b); float r1 = rop<MAX>(a, b);
  swap32(c, d); float r2 = rop<MAX>(c, d);
  swap16(r1, r2); float r = rop<MAX>(r1, r2);
  float u = r, v = r; swap16(u, v);
  a = u; b = u; swap32(a, b);
  c = v; d = v; swap32(c, d);
}
DI u32x4 widen(u32x2 a, u32x2 b) {
  auto r0 = __builtin_amdgcn_permlane16_swap(a[0], b[0], false, false);
  auto r1 = __builtin_amdgcn_permlane16_swap(a[1], b[1], false, false);
  u32x4 w = {r0[0], r1[0], r0[1], r1[1]};
  return w;
}
DI u32x2 pk4(const f32x4& v) { u32x2 w = {pk2(v[0], v[1]), pk2(v[2], v[3])}; return w; }

DI float max3(float a, float b, float c) { return fmaxf(fmaxf(a, b), c); }
DI float min3(float a, float b, float c) { return fminf(fminf(a, b), c); }
DI f32x4 exp4(const f32x4& p, float s, float o) {
  const f32x4 t = __builtin_elementwise_fma(p, (f32x4){s, s, s, s}, (f32x4){o, o, o, o});
  f32x4 e = {__builtin_amdgcn_exp2f(t[0]), __builtin_amdgcn_exp2f(t[1]), __builtin_amdgcn_exp2f(t[2]), __builtin_amdgcn_exp2f(t[3])};
  return e;
}
DI float sum8(const f32x4& a, const f32x4& b) { const f32x4 t = a + b; const f32x2 u = t.xy + t.zw; return u[0] + u[1]; }
DI void feature_map2(const f32x4 (&p)[2][2], bf16x8 (&fpos)[2], bf16x8 (&fneg)[2]) {
  const float L2E = 1.4426950408889634f;
  float mx[2], nm[2];
#pragma unroll
  for (int t = 0; t < 2; ++t) {
    const f32x4 &p0 = p[t][0], &p1 = p[t][1];
    mx[t] = max3(max3(max3(max3(p0[0], p0[1], p0[2]), p0[3], p1[0]), p1[1], p1[2]), p1[3], p0[0]);
    nm[t] = -min3(min3(min3(min3(p0[0], p0[1], p0[2]), p0[3], p1[0]), p1[1], p1[2]), p1[3], p0[0]);
  }
  xrow4<true>(mx[0], nm[0], mx[1], nm[1]);
  f32x4 e[2][2], n[2][2];
  float sp[2], sn[2];
#pragma unroll
  for (int t = 0; t < 2; ++t) {
    const float a = -mx[t] * L2E, b = -nm[t] * L2E;
#pragma unroll
    for (int h = 0; h < 2; ++h) { e[t][h] = exp4(p[t][h], L2E, a); n[t][h] = exp4(p[t][h], -L2E, b); }
    sp[t] = sum8(e[t][0], e[t][1]);
    sn[t] = sum8(n[t][0], n[t][1]);
  }
  xrow4<false>(sp[0], sn[0], sp[1], sn[1]);
#pragma unroll
  for (int t = 0; t < 2; ++t) {
    const float rp = __builtin_amdgcn_rcpf(sp[t]), rn = __builtin_amdgcn_rcpf(sn[t]);
    fpos[t] = pack8(e[t][0] * rp, e[t][1] * rp);
    fneg[t] = pack8(n[t][0] * rn, n[t][1] * rn);
  }
}

constexpr int K1_A0 = 0, K1_ABUF = 32768, K1_B0 = 65536, K1_BBUF = 40960, K1_WPHI = 147456, K1_DUMP = 147456 + 2048, K1_LDS = 147456 + 2048 + 2048;

constexpr int k1_pos(int i) { return i < 8 ? (i >> 1) + 4 * (i & 1) : 17 - i; }
constexpr unsigned SW = F_ * 2, SQ = 3 * D_ * 2;

struct K1Ctx {
  const char* sA;
  const char* sG; const char* sU; const char* sQ;
  const char* vP2; const char* vP3;
  unsigned stepP3;
  unsigned voffA0, voffA1, voffW, voffQ;
  const char* pf;
  unsigned ldsD;
  const char* vPF;
  unsigned stepPF;
  unsigned ldsA, ldsB;
};

DI void k1_warm(K1Ctx& c) {
  asm volatile("s_mov_b32 m0, %1\n\ts_nop 0\n\tglobal_load_lds_dword %0, off" ::"v"(c.vPF), "s"(c.ldsD) : "memory");
  padd(c.vPF, c.stepPF);
}
template <int BUF, bool PF = true> DI void k1_issue_A(K1Ctx& c) {
#pragma unroll
  for (int i = 0; i < 4; ++i) glds_s((i & 1) ? c.voffA1 : c.voffA0, c.sA + i * 16384, c.ldsA + BUF * K1_ABUF + i * 1024);
  c.sA += 128;
  if (PF) k1_warm(c);
}
template <int BUF> DI void k1_issue_B(K1Ctx& c) {
  const unsigned l = c.ldsB + BUF * K1_BBUF;
  glds_s(c.voffW, c.sG, l);             c.sG += 64 * SW;
  glds_s(c.voffW, c.sU, l + 8192);      c.sU += 64 * SW;
  glds_v(c.vP2, l + 2 * 8192);          c.vP2 += 64 * SW;
  glds_v(c.vP3, l + 3 * 8192);          padd(c.vP3, c.stepP3);
  glds_s(c.voffQ, c.sQ, l + 4 * 8192);  c.sQ += 64 * SQ;
}

template <int BUF, int KS> DI void k1_load(LAS char* lds, const unsigned (&aB)[4], const unsigned (&aA)[2], bf16x8 (&fb)[10], bf16x8 (&fa)[4]) {
#pragma unroll
  for (int m = 0; m < 4; ++m) fa[m] = *(LAS bf16x8*)(lds + aA[KS] + (K1_A0 + BUF * K1_ABUF + m * 2048));
#pragma unroll
  for (int i = 0; i < 10; ++i) fb[i] = tr_frag(lds + aB[k1_pos(i) & 3], (k1_pos(i) >> 2) * 8192 + BUF * K1_BBUF + KS * 4096);
}
template <bool FIRST = false>
DI void k1_mma(f32x4 (&acc)[10][4], const bf16x8 (&fb)[10], const bf16x8 (&fa)[4]) {
  __builtin_amdgcn_s_setprio(1);
#pragma unroll
  for (int i = 0; i < 10; ++i)
#pragma unroll
    for (int m = 0; m < 4; ++m) { if (FIRST) MFMA_ACC0(acc[i][m], fb[i], fa[m]); else MFMA_ACC(acc[i][m], fb[i], fa[m]); }
  __builtin_amdgcn_s_setprio(0);
}
template <int GRP, int BUF, int MODE, bool FIRST = false>
DI void k1_tile(LAS char* lds, K1Ctx& c, const unsigned (&aB)[4], const unsigned (&aA)[2], f32x4 (&acc)[10][4]) {
  bf16x8 fb[10], fa[4];
  k1_load<BUF, 0>(lds, aB, aA, fb, fa);
  if (MODE < 2) { if (GRP == 0) k1_issue_A<BUF ^ 1>(c); else k1_issue_B<BUF ^ 1>(c); WAIT_V(10); }
  else {
    WAIT_V(0);
    if (c.pf) asm volatile("s_mov_b32 m0, %2\n\ts_nop 0\n\tglobal_load_lds_dword %0, %1" ::"v"(__builtin_amdgcn_mbcnt_hi(~0u, __builtin_amdgcn_mbcnt_lo(~0u, 0u)) * 128u), "s"(c.pf), "s"(c.ldsD) : "memory");
  }
  WAIT_L0();
  BAR();
  k1_mma<FIRST>(acc, fb, fa);
  BAR();
  k1_load<BUF, 1>(lds, aB, aA, fb, fa);
  if (MODE == 0) { if (GRP == 0) k1_issue_B<BUF>(c); else k1_issue_A<BUF>(c); WAIT_V(10); }
  else if (MODE == 1) { WAIT_V(5); }
  WAIT_L0();
  BAR();
  k1_mma(acc, fb, fa);
  if (!(MODE == 2 && GRP == 1)) BAR();
}
template <int GRP>
DI void k1_main(LAS char* lds, K1Ctx& c, const unsigned (&aB)[4], const unsigned (&aA)[2], f32x4 (&acc)[10][4]) {
  if (GRP == 1) BAR();
  k1_tile<GRP, 0, 0, true>(lds, c, aB, aA, acc);
  k1_tile<GRP, 1, 0>(lds, c, aB, aA, acc);
#pragma unroll 1
  for (int t = 2; t < 14; t += 2) {
    c.stepPF &= t <= 10 - GRP ? ~0u : 0u;
    k1_tile<GRP, 0, 0>(lds, c, aB, aA, acc);
    c.stepPF &= t + 1 <= 10 - GRP ? ~0u : 0u;
    k1_tile<GRP, 1, 0>(lds, c, aB, aA, acc);
  }
  k1_tile<GRP, 0, 1>(lds, c, aB, aA, acc);
  k1_tile<GRP, 1, 2>(lds, c, aB, aA, acc);
  MFMA_DRAIN();
}

DI void epi_h(const f32x4 (&ga)[4], const f32x4 (&ua)[4], char* pp) {
#pragma unroll
  for (int mp = 0; mp < 2; ++mp) {
    u32x2 w[2];
#pragma unroll
    for (int h = 0; h < 2; ++h) {
      const int m = 2 * mp + h;
      w[h][0] = gelu_mul2(ga[m].xy, ua[m].xy);
      w[h][1] = gelu_mul2(ga[m].zw, ua[m].zw);
    }
    *(u32x4*)(pp + mp * 1024) = widen(w[0], w[1]);
  }
}
DI void epi_q(const f32x4 (&a0)[4], const f32x4 (&a1)[4], char* pp) {
#pragma unroll
  for (int mp = 0; mp < 2; ++mp) {
    *(u32x4*)(pp + mp * 1024) = widen(pk4(a0[2 * mp]), pk4(a0[2 * mp + 1]));
    *(u32x4*)(pp + PANEL + mp * 1024) = widen(pk4(a1[2 * mp]), pk4(a1[2 * mp + 1]));
  }
}
DI void epi_kv(const bf16x8 (&kp)[4], const bf16x8 (&vp)[4], LAS char* wphi  ,
               LAS char* scr  , LAS char* red  , int lane) {
  const int g = lane >> 4, c = lane & 15, q = (lane >> 2) & 3, p = lane & 3;
  bf16x8 wf[2];
#pragma unroll
  for (int fb = 0; fb < 2; ++fb) {
    LAS char* a = wphi + (4 * g + q) * 64 + fb * 32 + p * 8;
    s16x4 lo = __builtin_amdgcn_ds_read_tr16_b64_v4i16((LAS s16x4*)a);
    s16x4 hi = __builtin_amdgcn_ds_read_tr16_b64_v4i16((LAS s16x4*)(a + 1024));
    wf[fb] = __builtin_shufflevector(lo, hi, 0, 1, 2, 3, 4, 5, 6, 7);
  }
  const u32x4 onesu = {0x3F803F80u, 0x3F803F80u, 0x3F803F80u, 0x3F803F80u};
  const bf16x8 ONES = __builtin_bit_cast(bf16x8, onesu);
  const f32x4 Z = {0.f, 0.f, 0.f, 0.f};
  LAS char* wr = scr + c * 200 + g * 8;
  LAS char* rd = scr + (8 * g + q) * 200 + p * 8;
  f32x4 kv[4][3];
#pragma unroll
  for (int a = 0; a < 4; ++a)
#pragma unroll
    for (int b = 0; b < 3; ++b) kv[a][b] = Z;
#pragma unroll
  for (int mp = 0; mp < 2; ++mp) {
    f32x4 pp[2][2];
#pragma unroll
    for (int h = 0; h < 2; ++h) { pp[h][0] = MFMA(wf[0], kp[2 * mp + h], Z); pp[h][1] = MFMA(wf[1], kp[2 * mp + h], Z); }
    bf16x8 fp[2], fn[2];
    feature_map2(pp, fp, fn);
#pragma unroll
    for (int h = 0; h < 2; ++h) {
      const u32x4 a = __builtin_bit_cast(u32x4, fp[h]), b = __builtin_bit_cast(u32x4, fn[h]), v = __builtin_bit_cast(u32x4, vp[2 * mp + h]);
      LAS char* w = wr + h * 3200;
      *(LAS u32x2*)(w) = a.xy;       *(LAS u32x2*)(w + 32) = a.zw;
      *(LAS u32x2*)(w + 64) = b.xy;  *(LAS u32x2*)(w + 96) = b.zw;
      *(LAS u32x2*)(w + 128) = v.xy; *(LAS u32x2*)(w + 160) = v.zw;
    }
    bf16x8 fr[6];
#pragma unroll
    for (int a = 0; a < 6; ++a) {
      s16x4 lo = __builtin_amdgcn_ds_read_tr16_b64_v4i16((LAS s16x4*)(rd + a * 32));
      s16x4 hi = __builtin_amdgcn_ds_read_tr16_b64_v4i16((LAS s16x4*)(rd + a * 32 + 800));
      fr[a] = __builtin_shufflevector(lo, hi, 0, 1, 2, 3, 4, 5, 6, 7);
    }
#pragma unroll
    for (int a = 0; a < 4; ++a) {
      kv[a][0] = MFMA(fr[a], fr[4], kv[a][0]);
      kv[a][1] = MFMA(fr[a], fr[5], kv[a][1]);
      kv[a][2] = MFMA(fr[a], ONES, kv[a][2]);
    }
  }
#pragma unroll
  for (int a = 0; a < 4; ++a) {
    *(LAS f32x4*)(red + (a * 2 + 0) * 1024 + lane * 16) = kv[a][0];
    *(LAS f32x4*)(red + (a * 2 + 1) * 1024 + lane * 16) = kv[a][1];
    if (c == 0) *(LAS f32x4*)(red + 8192 + a * 64 + g * 16) = kv[a][2];
  }
}

__global__ __launch_bounds__(512, 2) void k1_kernel(const unsigned short* __restrict__ x, const unsigned short* __restrict__ wqkv,
                                                    const unsigned short* __restrict__ wphi, const unsigned short* __restrict__ wg,
                                                    const unsigned short* __restrict__ wu, const unsigned short* __restrict__ gatew,
                                                    char* __restrict__ act, char* __restrict__ qbuf,
                                                    float* __restrict__ kvp, float* __restrict__ gate, unsigned* __restrict__ flags,
                                                    const char* __restrict__ wd, const char* __restrict__ wo) {
  extern __shared__ __attribute__((aligned(16))) char smem[];
  LAS char* lds = (LAS char*)smem;
  const unsigned ldsb = (unsigned)(size_t)lds;
  const int tid = threadIdx.x, lane = tid & 63;
  const int wid = __builtin_amdgcn_readfirstlane(tid >> 6);
  const int grp = wid >> 2, j = wid & 3, wm = 2 * grp + (j >> 1), wn = (j & 1) ^ grp;
  const int bid = blockIdx.x, xcd = bid & 7, idx = bid >> 3;
  const int rt = idx & 7, cg = xcd * 4 + (idx >> 3);
  const int row0 = rt * 256;
  const int g = lane >> 4, c = lane & 15;

  STAMP_DECL;
  u32x4 gx[4], gw[2];
  if (grp == 1) {
    const char* xr = (const char*)x + (size_t)(bid * 8 + 2 * j) * 2048;
    const unsigned vo = lane * 16;
    asm volatile("global_load_dwordx4 %0, %6, %7\n\tglobal_load_dwordx4 %1, %6, %7 offset:1024\n\t"
                 "global_load_dwordx4 %2, %6, %7 offset:2048\n\tglobal_load_dwordx4 %3, %6, %7 offset:3072\n\t"
                 "global_load_dwordx4 %4, %6, %8\n\tglobal_load_dwordx4 %5, %6, %8 offset:1024"
                 : "=&v"(gx[0]), "=&v"(gx[1]), "=&v"(gx[2]), "=&v"(gx[3]), "=&v"(gw[0]), "=&v"(gw[1]) : "v"(vo), "s"(xr), "s"(gatew) : "memory");
  }

  K1Ctx cx;
  {
    const int ar = (grp == 0 ? 128 : 0) + 32 * j;
    cx.sA = (const char*)x + (unsigned)(row0 + ar) * 2048u;
    cx.ldsA = ldsb + K1_A0 + ar * 128;
    const int r = lane >> 3, ch = lane & 7;
    cx.voffA0 = r * 2048 + ((ch ^ (r >> 1)) * 16);
    cx.voffA1 = cx.voffA0 ^ 64;
    const unsigned kr = grp * 32 + 8 * j;
    const int l32 = (ch >> 1) ^ (((r >> 1) & 1) | ((j & 1) << 1));
    const unsigned h16 = (ch & 1) * 16;
    cx.sG = (const char*)wg + kr * SW + cg * 224;
    cx.sU = (const char*)wu + kr * SW + cg * 224;
    cx.sQ = (const char*)wqkv + kr * SQ + cg * 64;
    cx.voffW = r * SW + l32 * 32 + h16;
    cx.voffQ = r * SQ + (l32 < 2 ? 4096 + l32 * 32 : (l32 - 2) * 32) + h16;
    cx.vP2 = (l32 ? cx.sG + 96 + l32 * 32 : cx.sU + 128) + (r * SW + h16);
    cx.vP3 = l32 < 2 ? cx.sU + (160 + l32 * 32 + r * SW + h16) : cx.sQ + (2048 + (l32 - 2) * 32 + r * SQ + h16);
    cx.stepP3 = l32 < 2 ? 64 * SW : 64 * SQ;
    cx.ldsB = ldsb + K1_B0 + kr * 128;
    cx.ldsD = ldsb + K1_DUMP + wid * 256;
    const int id = wid * 256 + bid;
    cx.pf = id < 896 ? wd + (size_t)id * 8192 : (id < 1152 ? wo + (size_t)(id - 896) * 8192 : nullptr);
    {
      const unsigned e = wid * 13 + (lane < 12 ? lane : 12);
      const unsigned k2 = e >= 20 ? 1u : 0u, ln = e - 20 * k2, krow = 192 + idx * 2 + k2;
      cx.vPF = e >= 40 ? (const char*)x + ((idx * 64 + e - 40) * 2048u + 384u)
             : ln < 7  ? (const char*)wg + (krow * SW + xcd * 896 + ln * 128)
             : ln < 14 ? (const char*)wu + (krow * SW + xcd * 896 + (ln - 7) * 128)
                       : (const char*)wqkv + (krow * SQ + xcd * 256 + ((ln - 14) >> 1) * 2048 + ((ln - 14) & 1) * 128);
      cx.stepPF = e >= 40 ? 128u : (ln < 14 ? 64 * SW : 64 * SQ);
    }
    cx.sA = uni_ptr(cx.sA); cx.sG = uni_ptr(cx.sG); cx.sU = uni_ptr(cx.sU); cx.sQ = uni_ptr(cx.sQ);
    asm volatile("s_nop 4" : "+s"(cx.sA), "+s"(cx.sG), "+s"(cx.sU), "+s"(cx.sQ));
  }
  if (wid < 2) glds_s(lane * 16, (const char*)wphi + cg * 2048 + wid * 1024, ldsb + K1_WPHI + wid * 1024);
  if (grp == 0) k1_issue_B<0>(cx); else k1_issue_A<0, false>(cx);
  BAR();
  if (grp == 0) k1_issue_A<0, false>(cx); else k1_issue_B<0>(cx);

  unsigned aB[4], aA[2];
  {
    const int q = (lane >> 2) & 3, p = lane & 3;
    const int swz = ((q >> 1) & 1) | ((g & 1) << 1);
    const unsigned lb = K1_B0 + (8 * g + q) * 128 + p * 8;
#pragma unroll
    for (int i = 0; i < 4; ++i) aB[i] = lb + ((i ^ swz ^ (2 * wn)) * 32) + wn * (2 + (i >> 1)) * 8192;
    const int sa = (c >> 1) & 7;
    aA[0] = (64 * wm + c) * 128 + ((g ^ sa) * 16);
    aA[1] = aA[0] ^ 64;
  }

  f32x4 acc[10][4];

  WAIT_V(4);
  BAR();
  STAMP(1);
  if (grp == 0) k1_issue_B<1>(cx);
  else {
    k1_issue_A<1>(cx);
    asm volatile("" : "+v"(gx[0]), "+v"(gx[1]), "+v"(gx[2]), "+v"(gx[3]), "+v"(gw[0]), "+v"(gw[1]));
    float s0 = 0.f, s1 = 0.f;
#pragma unroll
    for (int h = 0; h < 2; ++h)
#pragma unroll
      for (int e = 0; e < 4; ++e) {
        const float wl = __uint_as_float(gw[h][e] << 16), wh = __uint_as_float(gw[h][e] & 0xffff0000u);
        s0 = __builtin_fmaf(__uint_as_float(gx[h][e] << 16), wl, s0);
        s0 = __builtin_fmaf(__uint_as_float(gx[h][e] & 0xffff0000u), wh, s0);
        s1 = __builtin_fmaf(__uint_as_float(gx[2 + h][e] << 16), wl, s1);
        s1 = __builtin_fmaf(__uint_as_float(gx[2 + h][e] & 0xffff0000u), wh, s1);
      }
    swap32(s0, s1);
    float sv = s0 + s1;
    { float t = sv; swap16(sv, t); sv += t; }
    sv += __uint_as_float(__builtin_amdgcn_update_dpp(0u, __float_as_uint(sv), 0x140, 0xf, 0xf, false));
    sv += __uint_as_float(__builtin_amdgcn_update_dpp(0u, __float_as_uint(sv), 0x141, 0xf, 0xf, false));
    sv += __uint_as_float(__builtin_amdgcn_update_dpp(0u, __float_as_uint(sv), 0x4e, 0xf, 0xf, false));
    sv += __uint_as_float(__builtin_amdgcn_update_dpp(0u, __float_as_uint(sv), 0xb1, 0xf, 0xf, false));
    const float gv = 1.0f / (1.0f + __expf(-sv));
    if ((lane & 31) == 0) {
      const float* gp = gate + bid * 8 + 2 * j;
      asm volatile("global_store_dword %0, %1, %2" ::"v"((unsigned)(lane >> 5) * 4u), "v"(gv), "s"(gp) : "memory");
    }
  }
  k1_warm(cx);
  if (grp == 0) k1_main<0>(lds, cx, aB, aA, acc); else k1_main<1>(lds, cx, aB, aA, acc);

  STAMP(2);
  int tz = threadIdx.x;
  asm volatile("" : "+v"(tz));
  const int lane2 = tz & 63, g2 = lane2 >> 4;
  if (tz < 8) flags[(16 + bid * 8 + tz) * 16] = 0u;
  if (bid == 0 && tz >= 64 && tz < 72) flags[(tz - 64) * 16] = 0u;
  const unsigned lo = (unsigned)(row0 + 64 * wm + 16 * (g2 & 1) + (lane2 & 15)) * 32u + (g2 >> 1) * 16u;
  char* hp = act + (size_t)(cg * 7 + 5 * wn) * PANEL + lo;
  if (wn == 0) {
#pragma unroll
    for (int sl = 0; sl < 5; ++sl) { epi_h(acc[2 * sl], acc[2 * sl + 1], hp + (size_t)sl * PANEL); SCHED(); }
  }
  int wn2 = wn;
  asm volatile("" : "+s"(wn2));
  if (wn2 != 0) {
    epi_q(acc[9], acc[8], qbuf + (size_t)(2 * cg) * PANEL + lo);
    SCHED();
    epi_h(acc[0], acc[4], hp);
    SCHED();
    epi_h(acc[2], acc[6], hp + PANEL);
    SCHED();
    STAMP(3);
    bf16x8 kp[4], vp[4];
#pragma unroll
    for (int m = 0; m < 4; ++m) { kp[m] = pack8(acc[1][m], acc[3][m]); vp[m] = pack8(acc[5][m], acc[7][m]); }
    epi_kv(kp, vp, lds + K1_WPHI, lds + 4 * 8448 + wm * 6400, lds + wm * 8448, lane2);
  }
  STAMP(4);
  __syncthreads();
  STAMP(5);
  for (int e = tz; e < 528; e += 512) {
    LAS char* p = lds + e * 16;
    const f32x4 s = (*(LAS f32x4*)p + *(LAS f32x4*)(p + 8448)) + (*(LAS f32x4*)(p + 2 * 8448) + *(LAS f32x4*)(p + 3 * 8448));
    *(f32x4*)(kvp + ((size_t)rt * 32 + cg) * KVP + e * 4) = s;
  }
  STAMP(6);
  STAMP_OUT((char*)gate + (1u << 20), wn);
}

constexpr int K2_ST = 32768, K2_NST = 4, K2_AUX = K2_ST * K2_NST, K2_LDS = K2_AUX + 8192;
constexpr int FL_OCNT = 0;
constexpr int FL_X = 16;
#define RLX_AGENT __ATOMIC_RELAXED, __HIP_MEMORY_SCOPE_AGENT

DI void glds_s1(unsigned voff, const void* sbase, unsigned ldsaddr) {
  asm volatile("s_mov_b32 m0, %2\n\ts_nop 0\n\tglobal_load_lds_dwordx4 %0, %1 sc1" ::"v"(voff), "s"(sbase), "s"(ldsaddr) : "memory");
}
DI void st16_sc1(void* p, u32x4 v) { asm volatile("global_store_dwordx4 %0, %1, off sc1\n\ts_nop 1" ::"v"(p), "v"(v) : "memory"); }
DI void wait_ge(const unsigned* p, unsigned need) {
  for (unsigned spins = 0; spins < (1u << 22); ++spins) {
    if ((unsigned)__builtin_amdgcn_readfirstlane(__hip_atomic_load(p, RLX_AGENT)) >= need) break;
    __builtin_amdgcn_s_sleep(1);
  }
}

DI f32x4 sel4(int cnd, f32x4 a, f32x4 b) { return cnd ? a : b; }

struct K2Ctx {
  const char* sA; const char* sB;
  unsigned voffB, vlin;
  unsigned ldsA, ldsB;
};
template <bool SC1> DI void k2_issue(K2Ctx& c, int slot) {
  const unsigned o = slot * K2_ST;
  if (SC1) { glds_s1(c.vlin, c.sA, c.ldsA + o); glds_s1(c.vlin, c.sA + 1024, c.ldsA + o + 1024); }
  else     { glds_s(c.vlin, c.sA, c.ldsA + o); glds_s(c.vlin, c.sA + 1024, c.ldsA + o + 1024); }
  glds_s(c.voffB, c.sB, c.ldsB + o);
  glds_s(c.voffB, c.sB + 128, c.ldsB + o + 8192);
  c.sA += 4 * PANEL;
  c.sB += 64 * 2048;
}
template <int SLOT> DI void k2_load(LAS char* lds, unsigned aA, const unsigned (&aB)[4], bf16x8 (&fb)[4], bf16x8 (&fa)[4]) {
#pragma unroll
  for (int m = 0; m < 4; ++m) fa[m] = *(LAS bf16x8*)(lds + aA + ((SLOT & 1) * K2_ST + m * 512));
#pragma unroll
  for (int i = 0; i < 4; ++i) fb[i] = tr_frag(lds + aB[i], (SLOT & 1) * K2_ST);
}
DI void k2_mma(f32x4 (&acc)[4][4], const bf16x8 (&fb)[4], const bf16x8 (&fa)[4]) {
  __builtin_amdgcn_s_setprio(1);
#pragma unroll
  for (int i = 0; i < 4; ++i)
#pragma unroll
    for (int m = 0; m < 4; ++m) MFMA_ACC(acc[i][m], fb[i], fa[m]);
  __builtin_amdgcn_s_setprio(0);
}
template <int SLOT, int ISSUE, int WAITN, bool LASTBAR>
DI void k2_step(LAS char* lds, K2Ctx& c, const unsigned (&aA)[2], const unsigned (&aB)[2][4], f32x4 (&acc)[4][4]) {
  bf16x8 fb[4], fa[4];
  k2_load<SLOT>(lds, aA[SLOT >> 1], aB[SLOT >> 1], fb, fa);
  if (ISSUE == 1) k2_issue<false>(c, (SLOT + 3) & 3);
  if (ISSUE == 2) k2_issue<true>(c, (SLOT + 3) & 3);
  asm volatile("s_waitcnt vmcnt(%0)" ::"i"(WAITN) : "memory");
  WAIT_L0();
  BAR();
  k2_mma(acc, fb, fa);
  if (LASTBAR) BAR();
}
template <int ISSUE> DI void k2_step4(LAS char* lds, K2Ctx& c, const unsigned (&aA)[2], const unsigned (&aB)[2][4], f32x4 (&acc)[4][4]) {
  k2_step<0, ISSUE, 8, true>(lds, c, aA, aB, acc);
  k2_step<1, ISSUE, 8, true>(lds, c, aA, aB, acc);
  k2_step<2, ISSUE, 8, true>(lds, c, aA, aB, acc);
  k2_step<3, ISSUE, 8, true>(lds, c, aA, aB, acc);
}

__global__ __launch_bounds__(512, 2) void k2_kernel(char* __restrict__ act, const char* __restrict__ qbuf,
                                                    const unsigned short* __restrict__ wphi, const char* __restrict__ wd,
                                                    const char* __restrict__ wo, const float* __restrict__ kvp,
                                                    const float* __restrict__ gate, char* __restrict__ xbuf,
                                                    unsigned* __restrict__ flags, unsigned short* __restrict__ out) {
  extern __shared__ __attribute__((aligned(16))) char smem[];
  LAS char* lds = (LAS char*)smem;
  const unsigned ldsb = (unsigned)(size_t)lds;
  const int tid = threadIdx.x, lane = tid & 63;
  const int wid = __builtin_amdgcn_readfirstlane(tid >> 6);
  const int grp = wid >> 2, j = wid & 3, wm = j >> 1, wn = j & 1;
  const int bid = blockIdx.x, xcd = bid & 7, idx = bid >> 3;
  const int ks = xcd >> 2, qd = xcd & 3;
  const int tm = (qd >> 1) * 8 + (idx & 7), tn = (qd & 1) * 4 + (idx >> 3);
  const int g = lane >> 4, c = lane & 15, q = (lane >> 2) & 3, p = lane & 3;

  STAMP_DECL;
  K2Ctx cx;
  {
    const int r = lane >> 3, ch = lane & 7;
    const int kb = 2 * grp + (j >> 1);
    cx.sA = act + (size_t)(ks * 144 + kb) * PANEL + (tm * 128 + (j & 1) * 64) * 32;
    cx.ldsA = ldsb + kb * 4096 + (j & 1) * 2048;
    cx.vlin = lane * 16;
    const int kr = 32 * grp + 8 * j;
    cx.sB = wd + (size_t)(ks * 2304 + kr) * 2048 + tn * 256;
    cx.voffB = r * 2048 + (((ch >> 1) ^ (((r >> 1) & 1) | ((j & 1) << 1))) * 32) + (ch & 1) * 16;
    cx.ldsB = ldsb + 16384 + kr * 128;
  }
  const int head = bid & 31, rb = bid >> 5;
  float og[4];
  {
    LAS char* aux = lds + K2_AUX;
    const char* kvb = (const char*)kvp + (size_t)head * (KVP * 4);
    constexpr unsigned RS = 32u * KVP * 4u;
    f32x4 zq[8], kq[8];
    if (wid == 0) {
      const unsigned vo = (512 + c) * 16;
      asm volatile("global_load_dwordx4 %0, %8, %9\n\tglobal_load_dwordx4 %1, %8, %10\n\tglobal_load_dwordx4 %2, %8, %11\n\tglobal_load_dwordx4 %3, %8, %12\n\t"
                   "global_load_dwordx4 %4, %8, %13\n\tglobal_load_dwordx4 %5, %8, %14\n\tglobal_load_dwordx4 %6, %8, %15\n\tglobal_load_dwordx4 %7, %8, %16"
                   : "=&v"(zq[0]), "=&v"(zq[1]), "=&v"(zq[2]), "=&v"(zq[3]), "=&v"(zq[4]), "=&v"(zq[5]), "=&v"(zq[6]), "=&v"(zq[7])
                   : "v"(vo), "s"(kvb), "s"(kvb + RS), "s"(kvb + 2 * RS), "s"(kvb + 3 * RS), "s"(kvb + 4 * RS), "s"(kvb + 5 * RS), "s"(kvb + 6 * RS), "s"(kvb + 7 * RS)
                   : "memory");
    } else if (wid <= 2) {
      glds_s(lane * 16, (const char*)wphi + (size_t)head * 2048 + (wid - 1) * 1024, ldsb + K2_AUX + 528 * 8 + (wid - 1) * 1024);
    }
    {
      const unsigned vo = tid * 16;
      asm volatile("global_load_dwordx4 %0, %8, %9\n\tglobal_load_dwordx4 %1, %8, %10\n\tglobal_load_dwordx4 %2, %8, %11\n\tglobal_load_dwordx4 %3, %8, %12\n\t"
                   "global_load_dwordx4 %4, %8, %13\n\tglobal_load_dwordx4 %5, %8, %14\n\tglobal_load_dwordx4 %6, %8, %15\n\tglobal_load_dwordx4 %7, %8, %16"
                   : "=&v"(kq[0]), "=&v"(kq[1]), "=&v"(kq[2]), "=&v"(kq[3]), "=&v"(kq[4]), "=&v"(kq[5]), "=&v"(kq[6]), "=&v"(kq[7])
                   : "v"(vo), "s"(kvb), "s"(kvb + RS), "s"(kvb + 2 * RS), "s"(kvb + 3 * RS), "s"(kvb + 4 * RS), "s"(kvb + 5 * RS), "s"(kvb + 6 * RS), "s"(kvb + 7 * RS)
                   : "memory");
    }
    bf16x8 qf[2];
    float gt[2];
    {
      const char* qb = qbuf + (size_t)(2 * head) * PANEL + (rb * 256 + wid * 32) * 32;
      const unsigned vq = (g >> 1) * PANEL + c * 32 + (g & 1) * 16, vg = c * 4;
      const float* g0 = gate + rb * 256 + wid * 32;
      const float* g1 = gate + tm * 128 + 64 * wm;
      asm volatile("global_load_dwordx4 %0, %8, %10\n\tglobal_load_dwordx4 %1, %8, %10 offset:512\n\t"
                   "global_load_dword %2, %9, %11\n\tglobal_load_dword %3, %9, %11 offset:64\n\t"
                   "global_load_dword %4, %9, %12\n\tglobal_load_dword %5, %9, %12 offset:64\n\t"
                   "global_load_dword %6, %9, %12 offset:128\n\tglobal_load_dword %7, %9, %12 offset:192"
                   : "=&v"(qf[0]), "=&v"(qf[1]), "=&v"(gt[0]), "=&v"(gt[1]), "=&v"(og[0]), "=&v"(og[1]), "=&v"(og[2]), "=&v"(og[3])
                   : "v"(vq), "v"(vg), "s"(qb), "s"(g0), "s"(g1) : "memory");
    }
    k2_issue<false>(cx, 0); k2_issue<false>(cx, 1); k2_issue<false>(cx, 2);
    asm volatile("s_waitcnt vmcnt(20)" : "+v"(kq[0]), "+v"(kq[1]), "+v"(kq[2]), "+v"(kq[3]), "+v"(kq[4]), "+v"(kq[5]), "+v"(kq[6]), "+v"(kq[7])::"memory");
    *(LAS u32x2*)(aux + tid * 8) = pk4(((kq[0] + kq[1]) + (kq[2] + kq[3])) + ((kq[4] + kq[5]) + (kq[6] + kq[7])));
    if (wid == 0) {
      asm volatile("" : "+v"(zq[0]), "+v"(zq[1]), "+v"(zq[2]), "+v"(zq[3]), "+v"(zq[4]), "+v"(zq[5]), "+v"(zq[6]), "+v"(zq[7]));
      *(LAS u32x2*)(aux + (512 + c) * 8) = pk4(((zq[0] + zq[1]) + (zq[2] + zq[3])) + ((zq[4] + zq[5]) + (zq[6] + zq[7])));
    }
    asm volatile("s_waitcnt vmcnt(12)" : "+v"(qf[0]), "+v"(qf[1]), "+v"(gt[0]), "+v"(gt[1]), "+v"(og[0]), "+v"(og[1]), "+v"(og[2]), "+v"(og[3])::"memory");
    WAIT_L0();
    BAR();
    bf16x8 wf[2];
#pragma unroll
    for (int fb = 0; fb < 2; ++fb) {
      LAS char* a = aux + 528 * 8 + (8 * g + q) * 64 + fb * 32 + p * 8;
      s16x4 lo = __builtin_amdgcn_ds_read_tr16_b64_v4i16((LAS s16x4*)a);
      s16x4 hi = __builtin_amdgcn_ds_read_tr16_b64_v4i16((LAS s16x4*)(a + 256));
      wf[fb] = __builtin_shufflevector(lo, hi, 0, 1, 2, 3, 4, 5, 6, 7);
    }
    bf16x8 kf[2][2], zf[2];
#pragma unroll
    for (int h = 0; h < 2; ++h) {
#pragma unroll
      for (int db = 0; db < 2; ++db) {
        const u32x2 lo = *(LAS u32x2*)(aux + (((2 * h) * 2 + db) * 64 + lane) * 8);
        const u32x2 hi = *(LAS u32x2*)(aux + (((2 * h + 1) * 2 + db) * 64 + lane) * 8);
        const u32x4 w = {lo[0], lo[1], hi[0], hi[1]};
        kf[h][db] = __builtin_bit_cast(bf16x8, w);
      }
      const u32x2 lo = *(LAS u32x2*)(aux + (512 + (2 * h) * 4 + g) * 8);
      const u32x2 hi = *(LAS u32x2*)(aux + (512 + (2 * h + 1) * 4 + g) * 8);
      const u32x4 w = {lo[0], lo[1], hi[0], hi[1]};
      zf[h] = __builtin_bit_cast(bf16x8, w);
    }
    const f32x4 Z = {0.f, 0.f, 0.f, 0.f};
    f32x4 pp[2][2];
#pragma unroll
    for (int m = 0; m < 2; ++m) { pp[m][0] = MFMA(wf[0], qf[m], Z); pp[m][1] = MFMA(wf[1], qf[m], Z); }
    bf16x8 fp[2], fn[2];
    feature_map2(pp, fp, fn);
    u32x2 w0[2], w1[2];
#pragma unroll
    for (int m = 0; m < 2; ++m) {
      f32x4 n0 = MFMA(kf[0][0], fp[m], Z); n0 = MFMA(kf[1][0], fn[m], n0);
      f32x4 n1 = MFMA(kf[0][1], fp[m], Z); n1 = MFMA(kf[1][1], fn[m], n1);
      f32x4 dn = MFMA(zf[0], fp[m], Z); dn = MFMA(zf[1], fn[m], dn);
      const float sc = gt[m] / (dn[0] + 1e-6f);
      w0[m] = pk4(n0 * sc); w1[m] = pk4(n1 * sc);
    }
    char* op = act + (size_t)(224 + 2 * head) * PANEL + (rb * 256 + wid * 32 + 16 * (g & 1) + c) * 32 + (g >> 1) * 16;
    st16_sc1(op, widen(w0[0], w0[1]));
    st16_sc1(op + PANEL, widen(w1[0], w1[1]));
  }

  STAMP(1);
  unsigned aA[2], aB[2][4];
  {
    aA[0] = (2 * grp + (g >> 1)) * 4096 + (64 * wm + c) * 32 + (g & 1) * 16;
    aA[1] = aA[0] + 2 * K2_ST;
    const int swz = ((q >> 1) & 1) | ((g & 1) << 1);
#pragma unroll
    for (int i = 0; i < 4; ++i) {
      aB[0][i] = 16384 + wn * 8192 + (32 * grp + 8 * g + q) * 128 + p * 8 + ((i ^ swz) * 32);
      aB[1][i] = aB[0][i] + 2 * K2_ST;
    }
  }
#pragma unroll
  for (int m = 0; m < 4; ++m) og[m] = 1.0f - og[m];
  f32x4 acc[4][4];
#pragma unroll
  for (int i = 0; i < 4; ++i)
#pragma unroll
    for (int m = 0; m < 4; ++m) acc[i][m] = (f32x4){0.f, 0.f, 0.f, 0.f};

  WAIT_V(10);
  BAR();
  if (grp == 1) BAR();
#pragma unroll 1
  for (int t = 0; t < 16; t += 4) {
    if (t == 4 && tid == 0) __hip_atomic_fetch_add(flags + (FL_OCNT + rb) * 16, 1u, RLX_AGENT);
    k2_step4<1>(lds, cx, aA, aB, acc);
  }
  k2_step<0, 1, 8, false>(lds, cx, aA, aB, acc);
  STAMP(2);
  if (ks == 1) {
    if (wid == 0) {
      wait_ge(flags + (FL_OCNT + (tm >> 1)) * 16, 32u);
    }
    cx.sB = wo + (32 * grp + 8 * j) * 2048 + tn * 256;
  }
  BAR();
  STAMP(3);
  k2_step<1, 2, 8, true>(lds, cx, aA, aB, acc);
  k2_step<2, 2, 8, true>(lds, cx, aA, aB, acc);
  k2_step<3, 2, 8, true>(lds, cx, aA, aB, acc);
  if (ks == 1) {
    MFMA_DRAIN();
#pragma unroll
    for (int i = 0; i < 4; ++i)
#pragma unroll
      for (int m = 0; m < 4; ++m) acc[i][m] *= og[m];
    SCHED(); asm volatile("s_nop 1"); SCHED();
  }
#pragma unroll 1
  for (int t = 20; t < 32; t += 4) k2_step4<2>(lds, cx, aA, aB, acc);
  k2_step<0, 2, 8, true>(lds, cx, aA, aB, acc);
  k2_step<1, 0, 4, true>(lds, cx, aA, aB, acc);
  k2_step<2, 0, 0, true>(lds, cx, aA, aB, acc);
  k2_step<3, 0, 0, false>(lds, cx, aA, aB, acc);
  STAMP(4);
  if (grp == 0) BAR();
  MFMA_DRAIN();
  if (ks == 0) {
#pragma unroll
    for (int i = 0; i < 4; ++i)
#pragma unroll
      for (int m = 0; m < 4; ++m) acc[i][m] *= og[m];
  }

  f32x4 mine[2][4];
  {
    LAS char* wr = lds + wid * 8192 + lane * 16;
    LAS char* rd = lds + (wid ^ 4) * 8192 + lane * 16;
#pragma unroll
    for (int i = 0; i < 2; ++i)
#pragma unroll
      for (int m = 0; m < 4; ++m) {
        *(LAS f32x4*)(wr + (i * 4 + m) * 1024) = sel4(grp, acc[i][m], acc[2 + i][m]);
        mine[i][m] = sel4(grp, acc[2 + i][m], acc[i][m]);
      }
    __syncthreads();
#pragma unroll
    for (int i = 0; i < 2; ++i)
#pragma unroll
      for (int m = 0; m < 4; ++m) mine[i][m] += *(LAS f32x4*)(rd + (i * 4 + m) * 1024);
  }
  STAMP(5);
  {
    char* xs = xbuf + ((size_t)bid * 8 + wid) * 4096 + lane * 16;
#pragma unroll
    for (int i = 0; i < 2; ++i)
#pragma unroll
      for (int m = 0; m < 2; ++m)
        st16_sc1(xs + (i * 2 + m) * 1024, __builtin_bit_cast(u32x4, sel4(ks, mine[i][m], mine[i][2 + m])));
    WAIT_V(0);
    if (lane == 0) __hip_atomic_store(flags + (FL_X + bid * 8 + wid) * 16, 1u, RLX_AGENT);
    wait_ge(flags + (FL_X + (bid ^ 4) * 8 + wid) * 16, 1u);
    __builtin_amdgcn_fence(__ATOMIC_ACQUIRE, "wavefront");
    STAMP(6);
    const char* xr = xbuf + ((size_t)(bid ^ 4) * 8 + wid) * 4096 + lane * 16;
    f32x4 o[4];
    asm volatile("global_load_dwordx4 %0, %4, off sc1\n\tglobal_load_dwordx4 %1, %4, off offset:1024 sc1\n\t"
                 "global_load_dwordx4 %2, %4, off offset:2048 sc1\n\tglobal_load_dwordx4 %3, %4, off offset:3072 sc1\n\t"
                 "s_waitcnt vmcnt(0)"
                 : "=&v"(o[0]), "=&v"(o[1]), "=&v"(o[2]), "=&v"(o[3]) : "v"(xr) : "memory");
#pragma unroll
    for (int m = 0; m < 2; ++m) {
      const f32x4 v0 = sel4(ks, mine[0][2 + m], mine[0][m]) + o[m];
      const f32x4 v1 = sel4(ks, mine[1][2 + m], mine[1][m]) + o[2 + m];
      const u32x4 w = widen(pk4(v0), pk4(v1));
      unsigned short* op = out + (size_t)(tm * 128 + 64 * wm + 32 * ks + 16 * m + c) * D_ + tn * 128 + 64 * wn + 32 * grp + 16 * (g & 1) + 8 * (g >> 1);
      *(u32x4*)op = w;
    }
  }
  STAMP_OUT((char*)gate + (2u << 20), ks);
}

extern "C" void kernel_launch(void* const* d_in, const int* in_sizes, int n_in, void* d_out, int out_size, void* d_ws,
                              size_t ws_size, hipStream_t stream) {
  const unsigned short* x = (const unsigned short*)d_in[0];
  const unsigned short* wqkv = (const unsigned short*)d_in[1];
  const unsigned short* wphi = (const unsigned short*)d_in[2];
  const char* wo = (const char*)d_in[3];
  const unsigned short* wg = (const unsigned short*)d_in[5];
  const unsigned short* wu = (const unsigned short*)d_in[6];
  const char* wd = (const char*)d_in[7];
  const unsigned short* gatew = (const unsigned short*)d_in[8];
  char* ws = (char*)d_ws;
  char* act = ws;
  char* qbuf = ws + (18u << 20);
  float* kvp = (float*)(ws + (22u << 20));
  char* xbuf = ws + (25u << 20) ;
  unsigned* flags = (unsigned*)(ws + (33u << 20));
  float* gate = (float*)(ws + (34u << 20));
  (void)hipFuncSetAttribute((const void*)k1_kernel, hipFuncAttributeMaxDynamicSharedMemorySize, K1_LDS);
  (void)hipFuncSetAttribute((const void*)k2_kernel, hipFuncAttributeMaxDynamicSharedMemorySize, K2_LDS);
  k1_kernel<<<256, 512, K1_LDS, stream>>>(x, wqkv, wphi, wg, wu, gatew, act, qbuf, kvp, gate, flags, wd, wo);
  k2_kernel<<<256, 512, K2_LDS, stream>>>(act, qbuf, wphi, wd, wo, kvp, gate, xbuf, flags, (unsigned short*)d_out);
}
```

```cpp
#include <hip/hip_runtime.h>
#include <hip/hip_bf16.h>
#include <stdint.h>

typedef short bf16x8 __attribute__((ext_vector_type(8)));
typedef short s16x4 __attribute__((ext_vector_type(4)));
typedef float f32x4 __attribute__((ext_vector_type(4)));
typedef float f32x2 __attribute__((ext_vector_type(2)));
typedef unsigned u32x4 __attribute__((ext_vector_type(4)));
typedef unsigned u32x2 __attribute__((ext_vector_type(2)));
typedef __bf16 bf16x2_t __attribute__((ext_vector_type(2)));
#define LAS __attribute__((address_space(3)))
#define GAS __attribute__((address_space(1)))
#define DI __device__ __forceinline__

constexpr int S_ = 2048, D_ = 1024, F_ = 3584;
constexpr int PANEL = S_ * 32;
constexpr int KVP = 4224;

DI void glds_s(unsigned voff, const void* sbase, unsigned ldsaddr) {
  asm volatile("s_mov_b32 m0, %2\n\ts_nop 0\n\tglobal_load_lds_dwordx4 %0, %1" ::"v"(voff), "s"(sbase), "s"(ldsaddr) : "memory");
}
DI void glds_v(const void* vaddr, unsigned ldsaddr) {
  asm volatile("s_mov_b32 m0, %1\n\ts_nop 0\n\tglobal_load_lds_dwordx4 %0, off" ::"v"(vaddr), "s"(ldsaddr) : "memory");
}
DI const char* uni_ptr(const char* p) {
  const unsigned long long v = (unsigned long long)p;
  const unsigned lo = __builtin_amdgcn_readfirstlane((unsigned)v), hi = __builtin_amdgcn_readfirstlane((unsigned)(v >> 32));
  return (const char*)(((unsigned long long)hi << 32) | lo);
}
DI void padd(const char*& p, unsigned step) {
  unsigned lo = (unsigned)(unsigned long long)p, hi = (unsigned)((unsigned long long)p >> 32);
  asm volatile("v_add_co_u32 %0, vcc, %0, %2\n\tv_addc_co_u32 %1, vcc, 0, %1, vcc" : "+v"(lo), "+v"(hi) : "v"(step) : "vcc");
  p = (const char*)(((unsigned long long)hi << 32) | lo);
}
#define WAIT_V(n) asm volatile("s_waitcnt vmcnt(" #n ")" ::: "memory")
#define WAIT_L0() __builtin_amdgcn_s_waitcnt(0xC07F)
#define SCHED() __builtin_amdgcn_sched_barrier(0)
#define BAR() do { SCHED(); asm volatile("" ::: "memory"); __builtin_amdgcn_s_barrier(); asm volatile("" ::: "memory"); SCHED(); } while (0)
#define MFMA_ACC(c, a, b) asm volatile("v_mfma_f32_16x16x32_bf16 %0, %1, %2, %0" : "+v"(c) : "v"(a), "v"(b))
#define MFMA_ACC0(c, a, b) asm volatile("v_mfma_f32_16x16x32_bf16 %0, %1, %2, 0" : "=v"(c) : "v"(a), "v"(b))
#define MFMA_DRAIN() do { SCHED(); asm volatile("s_nop 7\n\ts_nop 7" ::: "memory"); SCHED(); } while (0)
#define MFMA(a, b, c) __builtin_amdgcn_mfma_f32_16x16x32_bf16((a), (b), (c), 0, 0, 0)

DI unsigned pk2(float lo, float hi) {
  f32x2 v = {lo, hi};
  bf16x2_t r = __builtin_convertvector(v, bf16x2_t);
  return __builtin_bit_cast(unsigned, r);
}
DI bf16x8 pack8(const f32x4& a, const f32x4& b) {
  u32x4 p = {pk2(a[0], a[1]), pk2(a[2], a[3]), pk2(b[0], b[1]), pk2(b[2], b[3])};
  return __builtin_bit_cast(bf16x8, p);
}
DI bf16x8 tr_frag(LAS char* p, int off) {
  s16x4 a = __builtin_amdgcn_ds_read_tr16_b64_v4i16((LAS s16x4*)(p + off));
  s16x4 b = __builtin_amdgcn_ds_read_tr16_b64_v4i16((LAS s16x4*)(p + off + 512));
  return __builtin_shufflevector(a, b, 0, 1, 2, 3, 4, 5, 6, 7);
}
DI float bf2f(unsigned short u) { return __uint_as_float(((unsigned)u) << 16); }
DI unsigned gelu_mul2(f32x2 x, f32x2 u) {
  const float c0 = -2.3022081984f;
  const float c1 = -2.3022081984f * 0.044715f;
  const f32x2 p = __builtin_elementwise_fma(x * x, (f32x2){c1, c1}, (f32x2){c0, c0});
  const f32x2 a = x * p;
  const f32x2 e = {__builtin_amdgcn_exp2f(a[0]), __builtin_amdgcn_exp2f(a[1])};
  const f32x2 d = e + 1.0f;
  const f32x2 r = {__builtin_amdgcn_rcpf(d[0]), __builtin_amdgcn_rcpf(d[1])};
  const f32x2 o = (x * u) * r;
  return pk2(o[0], o[1]);
}
DI void swap16(float& a, float& b) {
  auto r = __builtin_amdgcn_permlane16_swap(__float_as_uint(a), __float_as_uint(b), false, false);
  a = __uint_as_float(r[0]); b = __uint_as_float(r[1]);
}
DI void swap32(float& a, float& b) {
  auto r = __builtin_amdgcn_permlane32_swap(__float_as_uint(a), __float_as_uint(b), false, false);
  a = __uint_as_float(r[0]); b = __uint_as_float(r[1]);
}
template <bool MAX> DI float rop(float a, float b) { return MAX ? fmaxf(a, b) : a + b; }
template <bool MAX> DI void xrow4(float& a, float& b, float& c, float& d) {
  swap32(a, b); float r1 = rop<MAX>(a, b);
  swap32(c, d); float r2 = rop<MAX>(c, d);
  swap16(r1, r2); float r = rop<MAX>(r1, r2);
  float u = r, v = r; swap16(u, v);
  a = u; b = u; swap32(a, b);
  c = v; d = v; swap32(c, d);
}
DI u32x4 widen(u32x2 a, u32x2 b) {
  auto r0 = __builtin_amdgcn_permlane16_swap(a[0], b[0], false, false);
  auto r1 = __builtin_amdgcn_permlane16_swap(a[1], b[1], false, false);
  u32x4 w = {r0[0], r1[0], r0[1], r1[1]};
  return w;
}
DI u32x2 pk4(const f32x4& v) { u32x2 w = {pk2(v[0], v[1]), pk2(v[2], v[3])}; return w; }

DI float max3(float a, float b, float c) { return fmaxf(fmaxf(a, b), c); }
DI float min3(float a, float b, float c) { return fminf(fminf(a, b), c); }
DI f32x4 exp4(const f32x4& p, float s, float o) {
  const f32x4 t = __builtin_elementwise_fma(p, (f32x4){s, s, s, s}, (f32x4){o, o, o, o});
  f32x4 e = {__builtin_amdgcn_exp2f(t[0]), __builtin_amdgcn_exp2f(t[1]), __builtin_amdgcn_exp2f(t[2]), __builtin_amdgcn_exp2f(t[3])};
  return e;
}
DI float sum8(const f32x4& a, const f32x4& b) { const f32x4 t = a + b; const f32x2 u = t.xy + t.zw; return u[0] + u[1]; }
DI void feature_map2(const f32x4 (&p)[2][2], bf16x8 (&fpos)[2], bf16x8 (&fneg)[2]) {
  const float L2E = 1.4426950408889634f;
  float mx[2], nm[2];
#pragma unroll
  for (int t = 0; t < 2; ++t) {
    const f32x4 &p0 = p[t][0], &p1 = p[t][1];
    mx[t] = max3(max3(max3(max3(p0[0], p0[1], p0[2]), p0[3], p1[0]), p1[1], p1[2]), p1[3], p0[0]);
    nm[t] = -min3(min3(min3(min3(p0[0], p0[1], p0[2]), p0[3], p1[0]), p1[1], p1[2]), p1[3], p0[0]);
  }
  xrow4<true>(mx[0], nm[0], mx[1], nm[1]);
  f32x4 e[2][2], n[2][2];
  float sp[2], sn[2];
#pragma unroll
  for (int t = 0; t < 2; ++t) {
    const float a = -mx[t] * L2E, b = -nm[t] * L2E;
#pragma unroll
    for (int h = 0; h < 2; ++h) { e[t][h] = exp4(p[t][h], L2E, a); n[t][h] = exp4(p[t][h], -L2E, b); }
    sp[t] = sum8(e[t][0], e[t][1]);
    sn[t] = sum8(n[t][0], n[t][1]);
  }
  xrow4<false>(sp[0], sn[0], sp[1], sn[1]);
#pragma unroll
  for (int t = 0; t < 2; ++t) {
    const float rp = __builtin_amdgcn_rcpf(sp[t]), rn = __builtin_amdgcn_rcpf(sn[t]);
    fpos[t] = pack8(e[t][0] * rp, e[t][1] * rp);
    fneg[t] = pack8(n[t][0] * rn, n[t][1] * rn);
  }
}

constexpr int K2_ST = 32768, K2_NST = 4, K2_AUX = K2_ST * K2_NST;
constexpr int FL_O = 512, FL_X = 1024, FL_DONE = 1024 + 2048 * 16;
#define RLX_AGENT __ATOMIC_RELAXED, __HIP_MEMORY_SCOPE_AGENT

DI void glds_s1(unsigned voff, const void* sbase, unsigned ldsaddr) {
  asm volatile("s_mov_b32 m0, %2\n\ts_nop 0\n\tglobal_load_lds_dwordx4 %0, %1 sc1" ::"v"(voff), "s"(sbase), "s"(ldsaddr) : "memory");
}
DI void st16_sc1(void* p, u32x4 v) { asm volatile("global_store_dwordx4 %0, %1, off sc1\n\ts_nop 1" ::"v"(p), "v"(v) : "memory"); }
DI void st8_sc1(void* p, u32x2 v) { asm volatile("global_store_dwordx2 %0, %1, off sc1" ::"v"(p), "v"(v) : "memory"); }
DI void wait_tok(const unsigned* p  , unsigned tok) {
  for (unsigned spins = 0; spins < (1u << 22); ++spins) {
    if (__all(__hip_atomic_load(p, RLX_AGENT) == tok)) break;
    __builtin_amdgcn_s_sleep(1);
  }
}

DI f32x4 sel4(int cnd, f32x4 a, f32x4 b) { return cnd ? a : b; }

#define LD8X4 "global_load_dwordx4 %0, %8, %9 sc1\n\tglobal_load_dwordx4 %1, %8, %10 sc1\n\tglobal_load_dwordx4 %2, %8, %11 sc1\n\tglobal_load_dwordx4 %3, %8, %12 sc1\n\t" \
              "global_load_dwordx4 %4, %8, %13 sc1\n\tglobal_load_dwordx4 %5, %8, %14 sc1\n\tglobal_load_dwordx4 %6, %8, %15 sc1\n\tglobal_load_dwordx4 %7, %8, %16 sc1\n\t" \
              "s_waitcnt vmcnt(0)"
DI u32x4 sum8bf(const u32x4 (&v)[8]) {
  u32x4 r;
#pragma unroll
  for (int e = 0; e < 4; ++e) {
    float lo = 0.f, hi = 0.f;
#pragma unroll
    for (int i = 0; i < 8; ++i) { lo += __uint_as_float(v[i][e] << 16); hi += __uint_as_float(v[i][e] & 0xffff0000u); }
    r[e] = pk2(lo, hi);
  }
  return r;
}

struct K2Ctx {
  const char* sA; const char* sB;
  unsigned voffB, vlin;
  unsigned ldsA, ldsB;
};
DI void k2_issueA(K2Ctx& c, int slot) {
  const unsigned o = slot * K2_ST;
  glds_s1(c.vlin, c.sA, c.ldsA + o); glds_s1(c.vlin, c.sA + 1024, c.ldsA + o + 1024);
  c.sA += 4 * PANEL;
}
DI void k2_issueB(K2Ctx& c, int slot) {
  const unsigned o = slot * K2_ST;
  glds_s(c.voffB, c.sB, c.ldsB + o); glds_s(c.voffB, c.sB + 128, c.ldsB + o + 8192);
  c.sB += 64 * 2048;
}
template <int SLOT> DI void k2_load(LAS char* lds, unsigned aA, const unsigned (&aB)[4], bf16x8 (&fb)[4], bf16x8 (&fa)[4]) {
#pragma unroll
  for (int m = 0; m < 4; ++m) fa[m] = *(LAS bf16x8*)(lds + aA + ((SLOT & 1) * K2_ST + m * 512));
#pragma unroll
  for (int i = 0; i < 4; ++i) fb[i] = tr_frag(lds + aB[i], (SLOT & 1) * K2_ST);
}
template <bool FIRST>
DI void k2_mma(f32x4 (&acc)[4][4], const bf16x8 (&fb)[4], const bf16x8 (&fa)[4]) {
  __builtin_amdgcn_s_setprio(1);
#pragma unroll
  for (int i = 0; i < 4; ++i)
#pragma unroll
    for (int m = 0; m < 4; ++m) { if (FIRST) MFMA_ACC0(acc[i][m], fb[i], fa[m]); else MFMA_ACC(acc[i][m], fb[i], fa[m]); }
  __builtin_amdgcn_s_setprio(0);
}
template <int SLOT, int ISSUE, int WAITN, bool LASTBAR, bool FIRST = false>
DI void k2_step(LAS char* lds, K2Ctx& c, const unsigned (&aA)[2], const unsigned (&aB)[2][4], f32x4 (&acc)[4][4]) {
  bf16x8 fb[4], fa[4];
  k2_load<SLOT>(lds, aA[SLOT >> 1], aB[SLOT >> 1], fb, fa);
  if (ISSUE) { k2_issueA(c, (SLOT + 3) & 3); k2_issueB(c, (SLOT + 3) & 3); }
  asm volatile("s_waitcnt vmcnt(%0)" ::"i"(WAITN) : "memory");
  WAIT_L0();
  BAR();
  k2_mma<FIRST>(acc, fb, fa);
  if (LASTBAR) BAR();
}
template <int ISSUE> DI void k2_step4(LAS char* lds, K2Ctx& c, const unsigned (&aA)[2], const unsigned (&aB)[2][4], f32x4 (&acc)[4][4]) {
  k2_step<0, ISSUE, 8, true>(lds, c, aA, aB, acc);
  k2_step<1, ISSUE, 8, true>(lds, c, aA, aB, acc);
  k2_step<2, ISSUE, 8, true>(lds, c, aA, aB, acc);
  k2_step<3, ISSUE, 8, true>(lds, c, aA, aB, acc);
}

constexpr int K1_A0 = 0, K1_ABUF = 32768, K1_B0 = 65536, K1_BBUF = 40960, K1_WPHI = 147456, K1_DUMP = 147456 + 2048, K1_LDS = 147456 + 2048 + 2048;

constexpr int k1_pos(int i) { return i < 8 ? (i >> 1) + 4 * (i & 1) : 17 - i; }
constexpr unsigned SW = F_ * 2, SQ = 3 * D_ * 2;

struct K1Ctx {
  const char* sA;
  const char* sG; const char* sU; const char* sQ;
  const char* vP2; const char* vP3;
  unsigned stepP3;
  unsigned voffA0, voffA1, voffW, voffQ;
  const char* pf;
  unsigned ldsD;
  const char* vPF;
  unsigned stepPF;
  unsigned ldsA, ldsB;
};

DI void k1_warm(K1Ctx& c) {
  asm volatile("s_mov_b32 m0, %1\n\ts_nop 0\n\tglobal_load_lds_dword %0, off" ::"v"(c.vPF), "s"(c.ldsD) : "memory");
  padd(c.vPF, c.stepPF);
}
template <int BUF, bool PF = true> DI void k1_issue_A(K1Ctx& c) {
#pragma unroll
  for (int i = 0; i < 4; ++i) glds_s((i & 1) ? c.voffA1 : c.voffA0, c.sA + i * 16384, c.ldsA + BUF * K1_ABUF + i * 1024);
  c.sA += 128;
  if (PF) k1_warm(c);
}
template <int BUF> DI void k1_issue_B(K1Ctx& c) {
  const unsigned l = c.ldsB + BUF * K1_BBUF;
  glds_s(c.voffW, c.sG, l);             c.sG += 64 * SW;
  glds_s(c.voffW, c.sU, l + 8192);      c.sU += 64 * SW;
  glds_v(c.vP2, l + 2 * 8192);          c.vP2 += 64 * SW;
  glds_v(c.vP3, l + 3 * 8192);          padd(c.vP3, c.stepP3);
  glds_s(c.voffQ, c.sQ, l + 4 * 8192);  c.sQ += 64 * SQ;
}

template <int BUF, int KS> DI void k1_load(LAS char* lds, const unsigned (&aB)[4], const unsigned (&aA)[2], bf16x8 (&fb)[10], bf16x8 (&fa)[4]) {
#pragma unroll
  for (int m = 0; m < 4; ++m) fa[m] = *(LAS bf16x8*)(lds + aA[KS] + (K1_A0 + BUF * K1_ABUF + m * 2048));
#pragma unroll
  for (int i = 0; i < 10; ++i) fb[i] = tr_frag(lds + aB[k1_pos(i) & 3], (k1_pos(i) >> 2) * 8192 + BUF * K1_BBUF + KS * 4096);
}
template <bool FIRST = false>
DI void k1_mma(f32x4 (&acc)[10][4], const bf16x8 (&fb)[10], const bf16x8 (&fa)[4]) {
  __builtin_amdgcn_s_setprio(1);
#pragma unroll
  for (int i = 0; i < 10; ++i)
#pragma unroll
    for (int m = 0; m < 4; ++m) { if (FIRST) MFMA_ACC0(acc[i][m], fb[i], fa[m]); else MFMA_ACC(acc[i][m], fb[i], fa[m]); }
  __builtin_amdgcn_s_setprio(0);
}
template <int GRP, int BUF, int MODE, bool FIRST = false>
DI void k1_tile(LAS char* lds, K1Ctx& c, const unsigned (&aB)[4], const unsigned (&aA)[2], f32x4 (&acc)[10][4]) {
  bf16x8 fb[10], fa[4];
  k1_load<BUF, 0>(lds, aB, aA, fb, fa);
  if (MODE < 2) { if (GRP == 0) k1_issue_A<BUF ^ 1>(c); else k1_issue_B<BUF ^ 1>(c); WAIT_V(10); }
  else {
    WAIT_V(0);
    if (c.pf) asm volatile("s_mov_b32 m0, %2\n\ts_nop 0\n\tglobal_load_lds_dword %0, %1" ::"v"(__builtin_amdgcn_mbcnt_hi(~0u, __builtin_amdgcn_mbcnt_lo(~0u, 0u)) * 128u), "s"(c.pf), "s"(c.ldsD) : "memory");
  }
  WAIT_L0();
  BAR();
  k1_mma<FIRST>(acc, fb, fa);
  BAR();
  k1_load<BUF, 1>(lds, aB, aA, fb, fa);
  if (MODE == 0) { if (GRP == 0) k1_issue_B<BUF>(c); else k1_issue_A<BUF>(c); WAIT_V(10); }
  else if (MODE == 1) { WAIT_V(5); }
  WAIT_L0();
  BAR();
  k1_mma(acc, fb, fa);
  if (!(MODE == 2 && GRP == 1)) BAR();
}
template <int GRP>
DI void k1_main(LAS char* lds, K1Ctx& c, const unsigned (&aB)[4], const unsigned (&aA)[2], f32x4 (&acc)[10][4]) {
  if (GRP == 1) BAR();
  k1_tile<GRP, 0, 0, true>(lds, c, aB, aA, acc);
  k1_tile<GRP, 1, 0>(lds, c, aB, aA, acc);
#pragma unroll 1
  for (int t = 2; t < 14; t += 2) {
    c.stepPF &= t <= 10 - GRP ? ~0u : 0u;
    k1_tile<GRP, 0, 0>(lds, c, aB, aA, acc);
    c.stepPF &= t + 1 <= 10 - GRP ? ~0u : 0u;
    k1_tile<GRP, 1, 0>(lds, c, aB, aA, acc);
  }
  k1_tile<GRP, 0, 1>(lds, c, aB, aA, acc);
  k1_tile<GRP, 1, 2>(lds, c, aB, aA, acc);
  MFMA_DRAIN();
}

DI void epi_h(const f32x4 (&ga)[4], const f32x4 (&ua)[4], char* pp) {
#pragma unroll
  for (int mp = 0; mp < 2; ++mp) {
    u32x2 w[2];
#pragma unroll
    for (int h = 0; h < 2; ++h) {
      const int m = 2 * mp + h;
      w[h][0] = gelu_mul2(ga[m].xy, ua[m].xy);
      w[h][1] = gelu_mul2(ga[m].zw, ua[m].zw);
    }
    st16_sc1(pp + mp * 1024, widen(w[0], w[1]));
  }
}
DI void load_wf(LAS char* wphi  , int lane, bf16x8 (&wf)[2]) {
  const int g = lane >> 4, q = (lane >> 2) & 3, p = lane & 3;
#pragma unroll
  for (int fb = 0; fb < 2; ++fb) {
    LAS char* a = wphi + (4 * g + q) * 64 + fb * 32 + p * 8;
    s16x4 lo = __builtin_amdgcn_ds_read_tr16_b64_v4i16((LAS s16x4*)a);
    s16x4 hi = __builtin_amdgcn_ds_read_tr16_b64_v4i16((LAS s16x4*)(a + 1024));
    wf[fb] = __builtin_shufflevector(lo, hi, 0, 1, 2, 3, 4, 5, 6, 7);
  }
}
DI void epi_kv(const bf16x8 (&kp)[4], const bf16x8 (&vp)[4], const bf16x8 (&wf)[2],
               LAS char* scr  , LAS char* red  , int lane) {
  const int g = lane >> 4, c = lane & 15, q = (lane >> 2) & 3, p = lane & 3;
  const u32x4 onesu = {0x3F803F80u, 0x3F803F80u, 0x3F803F80u, 0x3F803F80u};
  const bf16x8 ONES = __builtin_bit_cast(bf16x8, onesu);
  const f32x4 Z = {0.f, 0.f, 0.f, 0.f};
  LAS char* wr = scr + c * 200 + g * 8;
  LAS char* rd = scr + (8 * g + q) * 200 + p * 8;
  f32x4 kv[4][3];
#pragma unroll
  for (int a = 0; a < 4; ++a)
#pragma unroll
    for (int b = 0; b < 3; ++b) kv[a][b] = Z;
#pragma unroll
  for (int mp = 0; mp < 2; ++mp) {
    f32x4 pp[2][2];
#pragma unroll
    for (int h = 0; h < 2; ++h) { pp[h][0] = MFMA(wf[0], kp[2 * mp + h], Z); pp[h][1] = MFMA(wf[1], kp[2 * mp + h], Z); }
    bf16x8 fp[2], fn[2];
    feature_map2(pp, fp, fn);
#pragma unroll
    for (int h = 0; h < 2; ++h) {
      const u32x4 a = __builtin_bit_cast(u32x4, fp[h]), b = __builtin_bit_cast(u32x4, fn[h]), v = __builtin_bit_cast(u32x4, vp[2 * mp + h]);
      LAS char* w = wr + h * 3200;
      *(LAS u32x2*)(w) = a.xy;       *(LAS u32x2*)(w + 32) = a.zw;
      *(LAS u32x2*)(w + 64) = b.xy;  *(LAS u32x2*)(w + 96) = b.zw;
      *(LAS u32x2*)(w + 128) = v.xy; *(LAS u32x2*)(w + 160) = v.zw;
    }
    bf16x8 fr[6];
#pragma unroll
    for (int a = 0; a < 6; ++a) {
      s16x4 lo = __builtin_amdgcn_ds_read_tr16_b64_v4i16((LAS s16x4*)(rd + a * 32));
      s16x4 hi = __builtin_amdgcn_ds_read_tr16_b64_v4i16((LAS s16x4*)(rd + a * 32 + 800));
      fr[a] = __builtin_shufflevector(lo, hi, 0, 1, 2, 3, 4, 5, 6, 7);
    }
#pragma unroll
    for (int a = 0; a < 4; ++a) {
      kv[a][0] = MFMA(fr[a], fr[4], kv[a][0]);
      kv[a][1] = MFMA(fr[a], fr[5], kv[a][1]);
      kv[a][2] = MFMA(fr[a], ONES, kv[a][2]);
    }
  }
#pragma unroll
  for (int a = 0; a < 4; ++a) {
    *(LAS f32x4*)(red + (a * 2 + 0) * 1024 + lane * 16) = kv[a][0];
    *(LAS f32x4*)(red + (a * 2 + 1) * 1024 + lane * 16) = kv[a][1];
    if (c == 0) *(LAS f32x4*)(red + 8192 + a * 64 + g * 16) = kv[a][2];
  }
}

__global__ __launch_bounds__(512, 2) void fused_kernel(const unsigned short* __restrict__ x, const unsigned short* __restrict__ wqkv,
                                                       const unsigned short* __restrict__ wphi, const unsigned short* __restrict__ wg,
                                                       const unsigned short* __restrict__ wu, const unsigned short* __restrict__ gatew,
                                                       const char* __restrict__ wd, const char* __restrict__ wo,
                                                       char* act, char* kvp, float* gate, char* xbuf, unsigned* flags,
                                                       unsigned short* __restrict__ out) {
  extern __shared__ __attribute__((aligned(16))) char smem[];
  LAS char* lds = (LAS char*)smem;
  const unsigned ldsb = (unsigned)(size_t)lds;
  const int tid = threadIdx.x, lane = tid & 63;
  const int wid = __builtin_amdgcn_readfirstlane(tid >> 6);
  const int grp = wid >> 2, j = wid & 3, wm = 2 * grp + (j >> 1), wn = (j & 1) ^ grp;
  const int bid = blockIdx.x, xcd = bid & 7, idx = bid >> 3;
  const int rt = idx & 7, cg = xcd * 4 + (idx >> 3);
  const int row0 = rt * 256;
  const int g = lane >> 4, c = lane & 15;
  asm volatile("" ::"s"(x), "s"(wqkv), "s"(wphi), "s"(wg), "s"(wu), "s"(gatew));
  u32x4 gx[4], gw[2];
  if (grp == 1) {
    const char* xr = (const char*)x + (size_t)(bid * 8 + 2 * j) * 2048;
    const unsigned vo = lane * 16;
    asm volatile("global_load_dwordx4 %0, %6, %7\n\tglobal_load_dwordx4 %1, %6, %7 offset:1024\n\t"
                 "global_load_dwordx4 %2, %6, %7 offset:2048\n\tglobal_load_dwordx4 %3, %6, %7 offset:3072\n\t"
                 "global_load_dwordx4 %4, %6, %8\n\tglobal_load_dwordx4 %5, %6, %8 offset:1024"
                 : "=&v"(gx[0]), "=&v"(gx[1]), "=&v"(gx[2]), "=&v"(gx[3]), "=&v"(gw[0]), "=&v"(gw[1]) : "v"(vo), "s"(xr), "s"(gatew) : "memory");
  }

  K1Ctx cx;
  {
    const int ar = (grp == 0 ? 128 : 0) + 32 * j;
    cx.sA = (const char*)x + (unsigned)(row0 + ar) * 2048u;
    cx.ldsA = ldsb + K1_A0 + ar * 128;
    const int r = lane >> 3, ch = lane & 7;
    cx.voffA0 = r * 2048 + ((ch ^ (r >> 1)) * 16);
    cx.voffA1 = cx.voffA0 ^ 64;
    const unsigned kr = grp * 32 + 8 * j;
    const int l32 = (ch >> 1) ^ (((r >> 1) & 1) | ((j & 1) << 1));
    const unsigned h16 = (ch & 1) * 16;
    cx.sG = (const char*)wg + kr * SW + cg * 224;
    cx.sU = (const char*)wu + kr * SW + cg * 224;
    cx.sQ = (const char*)wqkv + kr * SQ + cg * 64;
    cx.voffW = r * SW + l32 * 32 + h16;
    cx.voffQ = r * SQ + (l32 < 2 ? 4096 + l32 * 32 : (l32 - 2) * 32) + h16;
    cx.vP2 = (l32 ? cx.sG + 96 + l32 * 32 : cx.sU + 128) + (r * SW + h16);
    cx.vP3 = l32 < 2 ? cx.sU + (160 + l32 * 32 + r * SW + h16) : cx.sQ + (2048 + (l32 - 2) * 32 + r * SQ + h16);
    cx.stepP3 = l32 < 2 ? 64 * SW : 64 * SQ;
    cx.ldsB = ldsb + K1_B0 + kr * 128;
    cx.sA = uni_ptr(cx.sA); cx.sG = uni_ptr(cx.sG); cx.sU = uni_ptr(cx.sU); cx.sQ = uni_ptr(cx.sQ);
    asm volatile("s_nop 4" : "+s"(cx.sA), "+s"(cx.sG), "+s"(cx.sU), "+s"(cx.sQ));
  }
  if (wid < 2) glds_s(lane * 16, (const char*)wphi + cg * 2048 + wid * 1024, ldsb + K1_WPHI + wid * 1024);
  if (grp == 0) k1_issue_B<0>(cx); else k1_issue_A<0, false>(cx);
  BAR();
  if (grp == 0) k1_issue_A<0, false>(cx); else k1_issue_B<0>(cx);
  {
    cx.ldsD = ldsb + K1_DUMP + wid * 256;
    const int id = wid * 256 + bid;
    cx.pf = id < 896 ? wd + (size_t)id * 8192 : (id < 1152 ? wo + (size_t)(id - 896) * 8192 : nullptr);
    {
      const unsigned e = wid * 5 + (lane < 4 ? lane : 4);
      const unsigned k2 = e >= 20 ? 1u : 0u, ln = e - 20 * k2, krow = 192 + idx * 2 + k2;
      cx.vPF = e >= 40 ? (const char*)x + ((idx * 64 + e - 40) * 2048u + 384u)
             : ln < 7  ? (const char*)wg + (krow * SW + xcd * 896 + ln * 128)
             : ln < 14 ? (const char*)wu + (krow * SW + xcd * 896 + (ln - 7) * 128)
                       : (const char*)wqkv + (krow * SQ + xcd * 256 + ((ln - 14) >> 1) * 2048 + ((ln - 14) & 1) * 128);
      cx.stepPF = e >= 40 ? 128u : (ln < 14 ? 64 * SW : 64 * SQ);
    }
  }

  unsigned aB[4], aA[2];
  {
    const int q = (lane >> 2) & 3, p = lane & 3;
    const int swz = ((q >> 1) & 1) | ((g & 1) << 1);
    const unsigned lb = K1_B0 + (8 * g + q) * 128 + p * 8;
#pragma unroll
    for (int i = 0; i < 4; ++i) aB[i] = lb + ((i ^ swz ^ (2 * wn)) * 32) + wn * (2 + (i >> 1)) * 8192;
    const int sa = (c >> 1) & 7;
    aA[0] = (64 * wm + c) * 128 + ((g ^ sa) * 16);
    aA[1] = aA[0] ^ 64;
  }

  f32x4 acc[10][4];

  unsigned tok;
  asm volatile("s_load_dword %0, %1, 0x0\n\ts_waitcnt lgkmcnt(0)" : "=s"(tok) : "s"(flags) : "memory");
  tok += 1u;
  WAIT_V(4);
  BAR();
  if (grp == 0) k1_issue_B<1>(cx);
  else {
    k1_issue_A<1>(cx);
    asm volatile("" : "+v"(gx[0]), "+v"(gx[1]), "+v"(gx[2]), "+v"(gx[3]), "+v"(gw[0]), "+v"(gw[1]));
    float s0 = 0.f, s1 = 0.f;
#pragma unroll
    for (int h = 0; h < 2; ++h)
#pragma unroll
      for (int e = 0; e < 4; ++e) {
        const float wl = __uint_as_float(gw[h][e] << 16), wh = __uint_as_float(gw[h][e] & 0xffff0000u);
        s0 = __builtin_fmaf(__uint_as_float(gx[h][e] << 16), wl, s0);
        s0 = __builtin_fmaf(__uint_as_float(gx[h][e] & 0xffff0000u), wh, s0);
        s1 = __builtin_fmaf(__uint_as_float(gx[2 + h][e] << 16), wl, s1);
        s1 = __builtin_fmaf(__uint_as_float(gx[2 + h][e] & 0xffff0000u), wh, s1);
      }
    swap32(s0, s1);
    float sv = s0 + s1;
    { float t = sv; swap16(sv, t); sv += t; }
    sv += __uint_as_float(__builtin_amdgcn_update_dpp(0u, __float_as_uint(sv), 0x140, 0xf, 0xf, false));
    sv += __uint_as_float(__builtin_amdgcn_update_dpp(0u, __float_as_uint(sv), 0x141, 0xf, 0xf, false));
    sv += __uint_as_float(__builtin_amdgcn_update_dpp(0u, __float_as_uint(sv), 0x4e, 0xf, 0xf, false));
    sv += __uint_as_float(__builtin_amdgcn_update_dpp(0u, __float_as_uint(sv), 0xb1, 0xf, 0xf, false));
    const float gv = 1.0f / (1.0f + __expf(-sv));
    if ((lane & 31) == 0) {
      const float* gp = gate + bid * 8 + 2 * j;
      asm volatile("global_store_dword %0, %1, %2 sc1" ::"v"((unsigned)(lane >> 5) * 4u), "v"(gv), "s"(gp) : "memory");
    }
  }
  k1_warm(cx);
  if (grp == 0) k1_main<0>(lds, cx, aB, aA, acc); else k1_main<1>(lds, cx, aB, aA, acc);

  int tz = threadIdx.x;
  asm volatile("" : "+v"(tz));
  const int lane2 = tz & 63, g2 = lane2 >> 4;
  const int c2 = lane2 & 15, q2 = (lane2 >> 2) & 3, p2 = lane2 & 3;
  const unsigned lo = (unsigned)(row0 + 64 * wm + 16 * (g2 & 1) + c2) * 32u + (g2 >> 1) * 16u;
  char* hp = act + (size_t)(cg * 7 + 5 * wn) * PANEL + lo;
  if (wn != 0) __builtin_amdgcn_s_setprio(2);
  if (wn == 0) {
#pragma unroll
    for (int sl = 0; sl < 5; ++sl) { epi_h(acc[2 * sl], acc[2 * sl + 1], hp + (size_t)sl * PANEL); SCHED(); }
  }
  int wn2 = wn;
  asm volatile("" : "+s"(wn2));
  bf16x8 qp[4], wf[2];
  if (wn2 != 0) {
    epi_h(acc[0], acc[4], hp);
    SCHED();
    epi_h(acc[2], acc[6], hp + PANEL);
    SCHED();
    bf16x8 kp[4], vp[4];
#pragma unroll
    for (int m = 0; m < 4; ++m) {
      kp[m] = pack8(acc[1][m], acc[3][m]); vp[m] = pack8(acc[5][m], acc[7][m]); qp[m] = pack8(acc[9][m], acc[8][m]);
    }
    load_wf(lds + K1_WPHI, lane2, wf);
    epi_kv(kp, vp, wf, lds + 4 * 8448 + wm * 6400, lds + wm * 8448, lane2);
    __builtin_amdgcn_s_setprio(0);
  }
  WAIT_L0();
  BAR();
  for (int e = tz; e < 528; e += 512) {
    LAS char* p = lds + e * 16;
    const f32x4 sv = (*(LAS f32x4*)p + *(LAS f32x4*)(p + 8448)) + (*(LAS f32x4*)(p + 2 * 8448) + *(LAS f32x4*)(p + 3 * 8448));
    st8_sc1(kvp + ((size_t)rt * 32 + cg) * KVP + e * 8, pk4(sv));
  }
  WAIT_V(0);
  BAR();
  if (tz < 32) __hip_atomic_store(flags + FL_DONE + tz * 256 + bid, tok, RLX_AGENT);

  const int wm2 = j >> 1, wnn = j & 1;
  const int ks = xcd >> 2, qd = xcd & 3;
  const int tm = (qd >> 1) * 8 + (idx & 7), tn = (qd & 1) * 4 + (idx >> 3);
  K2Ctx cy;
  {
    const int r = lane2 >> 3, ch = lane2 & 7;
    const int kb = 2 * grp + (j >> 1);
    cy.sA = act + (size_t)(ks * 144 + kb) * PANEL + (tm * 128 + (j & 1) * 64) * 32;
    cy.ldsA = ldsb + kb * 4096 + (j & 1) * 2048;
    cy.vlin = lane2 * 16;
    const int kr = 32 * grp + 8 * j;
    cy.sB = wd + (size_t)(ks * 2304 + kr) * 2048 + tn * 256;
    cy.voffB = r * 2048 + (((ch >> 1) ^ (((r >> 1) & 1) | ((j & 1) << 1))) * 32) + (ch & 1) * 16;
    cy.ldsB = ldsb + 16384 + kr * 128;
  }
  k2_issueB(cy, 0); k2_issueB(cy, 1); k2_issueB(cy, 2); k2_issueB(cy, 3);
  const f32x4 Z = {0.f, 0.f, 0.f, 0.f};
  bf16x8 fp[4], fn[4];
  int wn3 = wn;
  asm volatile("" : "+s"(wn3));
  if (wn3 != 0) {
#pragma unroll
    for (int mp = 0; mp < 2; ++mp) {
      f32x4 pp[2][2];
#pragma unroll
      for (int h = 0; h < 2; ++h) { pp[h][0] = MFMA(wf[0], qp[2 * mp + h], Z); pp[h][1] = MFMA(wf[1], qp[2 * mp + h], Z); }
      bf16x8 a[2], b[2];
      feature_map2(pp, a, b);
      fp[2 * mp] = a[0]; fp[2 * mp + 1] = a[1]; fn[2 * mp] = b[0]; fn[2 * mp + 1] = b[1];
    }
  }
  LAS char* aux = lds + K2_AUX;
  if (wn3 == 0) {
    const unsigned* rep = flags + FL_DONE + (bid >> 3) * 256;
    wait_tok(rep + xcd + 8 * ((lane2 & 7) + (idx & 24)), tok);
    const char* kvb = kvp + cg * KVP;
    constexpr unsigned RS = 32u * KVP;
    const unsigned vo = (wm * 64 + lane2) * 16;
    u32x4 kq[8];
    asm volatile(LD8X4 : "=&v"(kq[0]), "=&v"(kq[1]), "=&v"(kq[2]), "=&v"(kq[3]), "=&v"(kq[4]), "=&v"(kq[5]), "=&v"(kq[6]), "=&v"(kq[7])
                 : "v"(vo), "s"(kvb), "s"(kvb + RS), "s"(kvb + 2 * RS), "s"(kvb + 3 * RS), "s"(kvb + 4 * RS), "s"(kvb + 5 * RS), "s"(kvb + 6 * RS), "s"(kvb + 7 * RS)
                 : "memory");
    *(LAS u32x4*)(aux + vo) = sum8bf(kq);
    if (wid == 2) {
      const unsigned vz = 4096 + (lane2 & 7) * 16;
      asm volatile(LD8X4 : "=&v"(kq[0]), "=&v"(kq[1]), "=&v"(kq[2]), "=&v"(kq[3]), "=&v"(kq[4]), "=&v"(kq[5]), "=&v"(kq[6]), "=&v"(kq[7])
                   : "v"(vz), "s"(kvb), "s"(kvb + RS), "s"(kvb + 2 * RS), "s"(kvb + 3 * RS), "s"(kvb + 4 * RS), "s"(kvb + 5 * RS), "s"(kvb + 6 * RS), "s"(kvb + 7 * RS)
                   : "memory");
      *(LAS u32x4*)(aux + vz) = sum8bf(kq);
    }
    if (wid == 0) {
      const unsigned* df = rep + lane2 * 4;
      for (unsigned spins = 0; spins < (1u << 17); ++spins) {
        u32x4 v;
        asm volatile("global_load_dwordx4 %0, %1, off sc1\n\ts_waitcnt vmcnt(0)" : "=&v"(v) : "v"(df) : "memory");
        if (__all(v[0] == tok && v[1] == tok && v[2] == tok && v[3] == tok)) break;
        __builtin_amdgcn_s_sleep(1);
      }
      if (bid == 0 && lane2 == 0) __hip_atomic_store(flags, tok, RLX_AGENT);
    }
  }
  WAIT_L0();
  BAR();
  float og[4];
  {
    {
      const unsigned vg = c2 * 4;
      const float* g1 = gate + tm * 128 + 64 * wm2;
      asm volatile("global_load_dword %0, %4, %5 sc1\n\tglobal_load_dword %1, %4, %5 offset:64 sc1\n\t"
                   "global_load_dword %2, %4, %5 offset:128 sc1\n\tglobal_load_dword %3, %4, %5 offset:192 sc1"
                   : "=&v"(og[0]), "=&v"(og[1]), "=&v"(og[2]), "=&v"(og[3]) : "v"(vg), "s"(g1) : "memory");
    }
    k2_issueA(cy, 0); k2_issueA(cy, 1); k2_issueA(cy, 2); k2_issueA(cy, 3);
    int wn4 = wn;
    asm volatile("" : "+s"(wn4));
    if (wn4 != 0) {
      bf16x8 kf[2][2], zf[2];
#pragma unroll
      for (int h = 0; h < 2; ++h) {
#pragma unroll
        for (int db = 0; db < 2; ++db) {
          const u32x2 l0 = *(LAS u32x2*)(aux + (((2 * h) * 2 + db) * 64 + lane2) * 8);
          const u32x2 h0 = *(LAS u32x2*)(aux + (((2 * h + 1) * 2 + db) * 64 + lane2) * 8);
          const u32x4 w = {l0[0], l0[1], h0[0], h0[1]};
          kf[h][db] = __builtin_bit_cast(bf16x8, w);
        }
        const u32x2 l0 = *(LAS u32x2*)(aux + (512 + (2 * h) * 4 + g2) * 8);
        const u32x2 h0 = *(LAS u32x2*)(aux + (512 + (2 * h + 1) * 4 + g2) * 8);
        const u32x4 w = {l0[0], l0[1], h0[0], h0[1]};
        zf[h] = __builtin_bit_cast(bf16x8, w);
      }
      char* op = act + (size_t)(224 + 2 * cg) * PANEL + lo;
#pragma unroll
      for (int mp = 0; mp < 2; ++mp) {
        u32x2 w0[2], w1[2];
#pragma unroll
        for (int h = 0; h < 2; ++h) {
          const int m = 2 * mp + h;
          f32x4 n0 = MFMA(kf[0][0], fp[m], Z); n0 = MFMA(kf[1][0], fn[m], n0);
          f32x4 n1 = MFMA(kf[0][1], fp[m], Z); n1 = MFMA(kf[1][1], fn[m], n1);
          f32x4 dn = MFMA(zf[0], fp[m], Z); dn = MFMA(zf[1], fn[m], dn);
          const float sc = 1.0f / (dn[0] + 1e-6f);
          w0[h] = pk4(n0 * sc); w1[h] = pk4(n1 * sc);
        }
        st16_sc1(op + mp * 1024, widen(w0[0], w0[1]));
        st16_sc1(op + PANEL + mp * 1024, widen(w1[0], w1[1]));
      }
    }
  }

  unsigned bA[2], bB[2][4];
  {
    bA[0] = (2 * grp + (g2 >> 1)) * 4096 + (64 * wm2 + c2) * 32 + (g2 & 1) * 16;
    bA[1] = bA[0] + 2 * K2_ST;
    const int swz = ((q2 >> 1) & 1) | ((g2 & 1) << 1);
#pragma unroll
    for (int i = 0; i < 4; ++i) {
      bB[0][i] = 16384 + wnn * 8192 + (32 * grp + 8 * g2 + q2) * 128 + p2 * 8 + ((i ^ swz) * 32);
      bB[1][i] = bB[0][i] + 2 * K2_ST;
    }
  }
  f32x4 ac2[4][4], ac3[4][4];

  asm volatile("s_waitcnt vmcnt(6)" : "+v"(og[0]), "+v"(og[1]), "+v"(og[2]), "+v"(og[3])::"memory");
  BAR();
  if (grp == 1) BAR();
  k2_step<0, 0, 4, true, true>(lds, cy, bA, bB, ac2);
  k2_step<1, 1, 6, true>(lds, cy, bA, bB, ac2);
  k2_step<2, 1, 8, true>(lds, cy, bA, bB, ac2);
  k2_step<3, 1, 8, true>(lds, cy, bA, bB, ac2);
  if (tz == 0) __hip_atomic_store(flags + FL_O + rt * 32 + cg, tok, RLX_AGENT);
#pragma unroll 1
  for (int t = 4; t < 12; t += 4) k2_step4<1>(lds, cy, bA, bB, ac2);
  const unsigned* ofl = flags + FL_O + (tm >> 1) * 32 + (lane2 & 31);
  unsigned seen;
  asm volatile("global_load_dword %0, %1, off sc1" : "=&v"(seen) : "v"(ofl) : "memory");
  k2_step4<1>(lds, cy, bA, bB, ac2);
  k2_step<0, 1, 8, false>(lds, cy, bA, bB, ac2);
  asm volatile("" : "+v"(seen));
  if (ks == 1) {
    if (wid == 0 && !__all(seen == tok)) wait_tok(ofl, tok);
    cy.sB = wo + (32 * grp + 8 * j) * 2048 + tn * 256;
  }
  BAR();
  k2_step<1, 1, 8, true>(lds, cy, bA, bB, ac2);
  k2_step<2, 1, 8, true>(lds, cy, bA, bB, ac2);
  k2_step<3, 1, 8, true>(lds, cy, bA, bB, ac2);
  k2_step<0, 1, 8, true, true>(lds, cy, bA, bB, ac3);
  k2_step<1, 1, 8, true>(lds, cy, bA, bB, ac3);
  k2_step<2, 1, 8, true>(lds, cy, bA, bB, ac3);
  k2_step<3, 1, 8, true>(lds, cy, bA, bB, ac3);
#pragma unroll 1
  for (int t = 24; t < 32; t += 4) k2_step4<1>(lds, cy, bA, bB, ac3);
  k2_step<0, 1, 8, true>(lds, cy, bA, bB, ac3);
  k2_step<1, 0, 4, true>(lds, cy, bA, bB, ac3);
  k2_step<2, 0, 0, true>(lds, cy, bA, bB, ac3);
  k2_step<3, 0, 0, false>(lds, cy, bA, bB, ac3);
  MFMA_DRAIN();
#pragma unroll
  for (int m = 0; m < 4; ++m) {
    const float fa = 1.0f - og[m], fb = ks ? og[m] : fa;
#pragma unroll
    for (int i = 0; i < 4; ++i) ac2[i][m] = ac2[i][m] * fa + ac3[i][m] * fb;
  }
  if (grp == 0) BAR();
  f32x4 mine[2][2];
  {
    LAS char* wr = lds + wid * 8192 + lane2 * 16;
    LAS char* rd = lds + (wid ^ 4) * 8192 + lane2 * 16;
    f32x4 snd[2][2];
#pragma unroll
    for (int i = 0; i < 2; ++i)
#pragma unroll
      for (int m = 0; m < 2; ++m) {
        const f32x4 l0 = sel4(ks, ac2[i][m], ac2[i][2 + m]), h0 = sel4(ks, ac2[2 + i][m], ac2[2 + i][2 + m]);
        *(LAS f32x4*)(wr + (i * 2 + m) * 1024) = sel4(grp, l0, h0);
        snd[i][m] = sel4(grp, h0, l0);
      }
#pragma unroll
    for (int i = 0; i < 2; ++i)
#pragma unroll
      for (int m = 0; m < 2; ++m) {
        const f32x4 l0 = sel4(ks, ac2[i][2 + m], ac2[i][m]), h0 = sel4(ks, ac2[2 + i][2 + m], ac2[2 + i][m]);
        *(LAS f32x4*)(wr + 4096 + (i * 2 + m) * 1024) = sel4(grp, l0, h0);
        mine[i][m] = sel4(grp, h0, l0);
      }
    WAIT_L0();
    BAR();
    char* xs = xbuf + ((size_t)bid * 8 + wid) * 4096 + lane2 * 16;
#pragma unroll
    for (int i = 0; i < 2; ++i)
#pragma unroll
      for (int m = 0; m < 2; ++m)
        st16_sc1(xs + (i * 2 + m) * 1024, __builtin_bit_cast(u32x4, snd[i][m] + *(LAS f32x4*)(rd + (i * 2 + m) * 1024)));
#pragma unroll
    for (int i = 0; i < 2; ++i)
#pragma unroll
      for (int m = 0; m < 2; ++m) mine[i][m] += *(LAS f32x4*)(rd + 4096 + (i * 2 + m) * 1024);
  }
  {
    WAIT_V(0);
    if (lane2 == 0) __hip_atomic_store(flags + FL_X + (bid * 8 + wid) * 16, tok, RLX_AGENT);
    wait_tok(flags + FL_X + ((bid ^ 4) * 8 + wid) * 16, tok);
    __builtin_amdgcn_fence(__ATOMIC_ACQUIRE, "wavefront");
    const char* xr = xbuf + ((size_t)(bid ^ 4) * 8 + wid) * 4096 + lane2 * 16;
    f32x4 o[4];
    asm volatile("global_load_dwordx4 %0, %4, off sc1\n\tglobal_load_dwordx4 %1, %4, off offset:1024 sc1\n\t"
                 "global_load_dwordx4 %2, %4, off offset:2048 sc1\n\tglobal_load_dwordx4 %3, %4, off offset:3072 sc1\n\t"
                 "s_waitcnt vmcnt(0)"
                 : "=&v"(o[0]), "=&v"(o[1]), "=&v"(o[2]), "=&v"(o[3]) : "v"(xr) : "memory");
#pragma unroll
    for (int m = 0; m < 2; ++m) {
      const f32x4 v0 = mine[0][m] + o[m];
      const f32x4 v1 = mine[1][m] + o[2 + m];
      const u32x4 w = widen(pk4(v0), pk4(v1));
      unsigned short* op = out + (size_t)(tm * 128 + 64 * wm2 + 32 * ks + 16 * m + c2) * D_ + tn * 128 + 64 * wnn + 32 * grp + 16 * (g2 & 1) + 8 * (g2 >> 1);
      *(u32x4*)op = w;
    }
  }
}

extern "C" void kernel_launch(void* const* d_in, const int* in_sizes, int n_in, void* d_out, int out_size, void* d_ws,
                              size_t ws_size, hipStream_t stream) {
  const unsigned short* x = (const unsigned short*)d_in[0];
  const unsigned short* wqkv = (const unsigned short*)d_in[1];
  const unsigned short* wphi = (const unsigned short*)d_in[2];
  const char* wo = (const char*)d_in[3];
  const unsigned short* wg = (const unsigned short*)d_in[5];
  const unsigned short* wu = (const unsigned short*)d_in[6];
  const char* wd = (const char*)d_in[7];
  const unsigned short* gatew = (const unsigned short*)d_in[8];
  char* ws = (char*)d_ws;
  char* act = ws;
  char* xbuf = ws + (18u << 20);
  char* kvp = ws + (26u << 20);
  unsigned* flags = (unsigned*)(ws + (26u << 20) + (1280u << 10));
  float* gate = (float*)(ws + (26u << 20) + (1536u << 10));
  (void)hipFuncSetAttribute((const void*)fused_kernel, hipFuncAttributeMaxDynamicSharedMemorySize, K1_LDS);
  fused_kernel<<<256, 512, K1_LDS, stream>>>(x, wqkv, wphi, wg, wu, gatew, wd, wo, act, kvp, gate, xbuf, flags, (unsigned short*)d_out);
}
```

```cpp
#include <hip/hip_runtime.h>
#include <hip/hip_bf16.h>
#include <stdint.h>

typedef short bf16x8 __attribute__((ext_vector_type(8)));
typedef short s16x4 __attribute__((ext_vector_type(4)));
typedef float f32x4 __attribute__((ext_vector_type(4)));
typedef float f32x2 __attribute__((ext_vector_type(2)));
typedef unsigned u32x4 __attribute__((ext_vector_type(4)));
typedef unsigned u32x2 __attribute__((ext_vector_type(2)));
typedef __bf16 bf16x2_t __attribute__((ext_vector_type(2)));
#define LAS __attribute__((address_space(3)))
#define GAS __attribute__((address_space(1)))
#define DI __device__ __forceinline__

constexpr int S_ = 2048, D_ = 1024, F_ = 3584;
constexpr int PANEL = S_ * 32;
constexpr int KVP = 4224;

DI void glds_s(unsigned voff, const void* sbase, unsigned ldsaddr) {
  asm volatile("s_mov_b32 m0, %2\n\ts_nop 0\n\tglobal_load_lds_dwordx4 %0, %1" ::"v"(voff), "s"(sbase), "s"(ldsaddr) : "memory");
}
DI void glds_v(const void* vaddr, unsigned ldsaddr) {
  asm volatile("s_mov_b32 m0, %1\n\ts_nop 0\n\tglobal_load_lds_dwordx4 %0, off" ::"v"(vaddr), "s"(ldsaddr) : "memory");
}
DI const char* uni_ptr(const char* p) {
  const unsigned long long v = (unsigned long long)p;
  const unsigned lo = __builtin_amdgcn_readfirstlane((unsigned)v), hi = __builtin_amdgcn_readfirstlane((unsigned)(v >> 32));
  return (const char*)(((unsigned long long)hi << 32) | lo);
}
DI void padd(const char*& p, unsigned step) {
  unsigned lo = (unsigned)(unsigned long long)p, hi = (unsigned)((unsigned long long)p >> 32);
  asm volatile("v_add_co_u32 %0, vcc, %0, %2\n\tv_addc_co_u32 %1, vcc, 0, %1, vcc" : "+v"(lo), "+v"(hi) : "v"(step) : "vcc");
  p = (const char*)(((unsigned long long)hi << 32) | lo);
}
#define WAIT_V(n) asm volatile("s_waitcnt vmcnt(" #n ")" ::: "memory")
#define WAIT_L0() __builtin_amdgcn_s_waitcnt(0xC07F)
#define SCHED() __builtin_amdgcn_sched_barrier(0)
#define BAR() do { SCHED(); asm volatile("" ::: "memory"); __builtin_amdgcn_s_barrier(); asm volatile("" ::: "memory"); SCHED(); } while (0)
#define MFMA_ACC(c, a, b) asm volatile("v_mfma_f32_16x16x32_bf16 %0, %1, %2, %0" : "+v"(c) : "v"(a), "v"(b))
#define MFMA_ACC0(c, a, b) asm volatile("v_mfma_f32_16x16x32_bf16 %0, %1, %2, 0" : "=v"(c) : "v"(a), "v"(b))
#define MFMA_DRAIN() do { SCHED(); asm volatile("s_nop 7\n\ts_nop 7" ::: "memory"); SCHED(); } while (0)
#define MFMA(a, b, c) __builtin_amdgcn_mfma_f32_16x16x32_bf16((a), (b), (c), 0, 0, 0)

DI unsigned pk2(float lo, float hi) {
  f32x2 v = {lo, hi};
  bf16x2_t r = __builtin_convertvector(v, bf16x2_t);
  return __builtin_bit_cast(unsigned, r);
}
DI bf16x8 pack8(const f32x4& a, const f32x4& b) {
  u32x4 p = {pk2(a[0], a[1]), pk2(a[2], a[3]), pk2(b[0], b[1]), pk2(b[2], b[3])};
  return __builtin_bit_cast(bf16x8, p);
}
DI bf16x8 tr_frag(LAS char* p, int off) {
  s16x4 a = __builtin_amdgcn_ds_read_tr16_b64_v4i16((LAS s16x4*)(p + off));
  s16x4 b = __builtin_amdgcn_ds_read_tr16_b64_v4i16((LAS s16x4*)(p + off + 512));
  return __builtin_shufflevector(a, b, 0, 1, 2, 3, 4, 5, 6, 7);
}
DI float bf2f(unsigned short u) { return __uint_as_float(((unsigned)u) << 16); }
DI unsigned gelu_mul2(f32x2 x, f32x2 u) {
  const float c0 = -2.3022081984f;
  const float c1 = -2.3022081984f * 0.044715f;
  const f32x2 p = __builtin_elementwise_fma(x * x, (f32x2){c1, c1}, (f32x2){c0, c0});
  const f32x2 a = x * p;
  const f32x2 e = {__builtin_amdgcn_exp2f(a[0]), __builtin_amdgcn_exp2f(a[1])};
  const f32x2 d = e + 1.0f;
  const f32x2 r = {__builtin_amdgcn_rcpf(d[0]), __builtin_amdgcn_rcpf(d[1])};
  const f32x2 o = (x * u) * r;
  return pk2(o[0], o[1]);
}
DI void swap16(float& a, float& b) {
  auto r = __builtin_amdgcn_permlane16_swap(__float_as_uint(a), __float_as_uint(b), false, false);
  a = __uint_as_float(r[0]); b = __uint_as_float(r[1]);
}
DI void swap32(float& a, float& b) {
  auto r = __builtin_amdgcn_permlane32_swap(__float_as_uint(a), __float_as_uint(b), false, false);
  a = __uint_as_float(r[0]); b = __uint_as_float(r[1]);
}
template <bool MAX> DI float rop(float a, float b) { return MAX ? fmaxf(a, b) : a + b; }
template <bool MAX> DI void xrow4(float& a, float& b, float& c, float& d) {
  swap32(a, b); float r1 = rop<MAX>(a, b);
  swap32(c, d); float r2 = rop<MAX>(c, d);
  swap16(r1, r2); float r = rop<MAX>(r1, r2);
  float u = r, v = r; swap16(u, v);
  a = u; b = u; swap32(a, b);
  c = v; d = v; swap32(c, d);
}
DI u32x4 widen(u32x2 a, u32x2 b) {
  auto r0 = __builtin_amdgcn_permlane16_swap(a[0], b[0], false, false);
  auto r1 = __builtin_amdgcn_permlane16_swap(a[1], b[1], false, false);
  u32x4 w = {r0[0], r1[0], r0[1], r1[1]};
  return w;
}
DI u32x2 pk4(const f32x4& v) { u32x2 w = {pk2(v[0], v[1]), pk2(v[2], v[3])}; return w; }

DI float max3(float a, float b, float c) { return fmaxf(fmaxf(a, b), c); }
DI float min3(float a, float b, float c) { return fminf(fminf(a, b), c); }
DI f32x4 exp4(const f32x4& p, float s, float o) {
  const f32x4 t = __builtin_elementwise_fma(p, (f32x4){s, s, s, s}, (f32x4){o, o, o, o});
  f32x4 e = {__builtin_amdgcn_exp2f(t[0]), __builtin_amdgcn_exp2f(t[1]), __builtin_amdgcn_exp2f(t[2]), __builtin_amdgcn_exp2f(t[3])};
  return e;
}
DI float sum8(const f32x4& a, const f32x4& b) { const f32x4 t = a + b; const f32x2 u = t.xy + t.zw; return u[0] + u[1]; }
DI void feature_map2(const f32x4 (&p)[2][2], bf16x8 (&fpos)[2], bf16x8 (&fneg)[2]) {
  const float L2E = 1.4426950408889634f;
  float mx[2], nm[2];
#pragma unroll
  for (int t = 0; t < 2; ++t) {
    const f32x4 &p0 = p[t][0], &p1 = p[t][1];
    mx[t] = max3(max3(max3(max3(p0[0], p0[1], p0[2]), p0[3], p1[0]), p1[1], p1[2]), p1[3], p0[0]);
    nm[t] = -min3(min3(min3(min3(p0[0], p0[1], p0[2]), p0[3], p1[0]), p1[1], p1[2]), p1[3], p0[0]);
  }
  xrow4<true>(mx[0], nm[0], mx[1], nm[1]);
  f32x4 e[2][2], n[2][2];
  float sp[2], sn[2];
#pragma unroll
  for (int t = 0; t < 2; ++t) {
    const float a = -mx[t] * L2E, b = -nm[t] * L2E;
#pragma unroll
    for (int h = 0; h < 2; ++h) { e[t][h] = exp4(p[t][h], L2E, a); n[t][h] = exp4(p[t][h], -L2E, b); }
    sp[t] = sum8(e[t][0], e[t][1]);
    sn[t] = sum8(n[t][0], n[t][1]);
  }
  xrow4<false>(sp[0], sn[0], sp[1], sn[1]);
#pragma unroll
  for (int t = 0; t < 2; ++t) {
    const float rp = __builtin_amdgcn_rcpf(sp[t]), rn = __builtin_amdgcn_rcpf(sn[t]);
    fpos[t] = pack8(e[t][0] * rp, e[t][1] * rp);
    fneg[t] = pack8(n[t][0] * rn, n[t][1] * rn);
  }
}

constexpr int K2_ST = 32768, K2_NST = 4, K2_AUX = K2_ST * K2_NST;
constexpr int FL_O = 512, FL_X = 1024, FL_DONE = 1024 + 2048 * 16;
#define RLX_AGENT __ATOMIC_RELAXED, __HIP_MEMORY_SCOPE_AGENT

DI void glds_s1(unsigned voff, const void* sbase, unsigned ldsaddr) {
  asm volatile("s_mov_b32 m0, %2\n\ts_nop 0\n\tglobal_load_lds_dwordx4 %0, %1 sc1" ::"v"(voff), "s"(sbase), "s"(ldsaddr) : "memory");
}
DI void st16_sc1(void* p, u32x4 v) { asm volatile("global_store_dwordx4 %0, %1, off sc1\n\ts_nop 1" ::"v"(p), "v"(v) : "memory"); }
DI void st8_sc1(void* p, u32x2 v) { asm volatile("global_store_dwordx2 %0, %1, off sc1" ::"v"(p), "v"(v) : "memory"); }
DI void wait_tok(const unsigned* p  , unsigned tok) {
  for (unsigned spins = 0; spins < (1u << 17); ++spins) {
    if (__all(__hip_atomic_load(p, RLX_AGENT) == tok)) break;
    __builtin_amdgcn_s_sleep(1);
  }
}

DI f32x4 sel4(int cnd, f32x4 a, f32x4 b) { return cnd ? a : b; }

#define LD8X4 "global_load_dwordx4 %0, %8, %9 sc1\n\tglobal_load_dwordx4 %1, %8, %10 sc1\n\tglobal_load_dwordx4 %2, %8, %11 sc1\n\tglobal_load_dwordx4 %3, %8, %12 sc1\n\t" \
              "global_load_dwordx4 %4, %8, %13 sc1\n\tglobal_load_dwordx4 %5, %8, %14 sc1\n\tglobal_load_dwordx4 %6, %8, %15 sc1\n\tglobal_load_dwordx4 %7, %8, %16 sc1\n\t" \
              "s_waitcnt vmcnt(0)"
DI u32x4 sum8bf(const u32x4 (&v)[8]) {
  u32x4 r;
#pragma unroll
  for (int e = 0; e < 4; ++e) {
    float lo = 0.f, hi = 0.f;
#pragma unroll
    for (int i = 0; i < 8; ++i) { lo += __uint_as_float(v[i][e] << 16); hi += __uint_as_float(v[i][e] & 0xffff0000u); }
    r[e] = pk2(lo, hi);
  }
  return r;
}

struct K2Ctx {
  const char* sA; const char* sB;
  unsigned voffB, vlin;
  unsigned ldsA, ldsB;
};
DI void k2_issueA(K2Ctx& c, int slot) {
  const unsigned o = slot * K2_ST;
  glds_s1(c.vlin, c.sA, c.ldsA + o); glds_s1(c.vlin, c.sA + 1024, c.ldsA + o + 1024);
  c.sA += 4 * PANEL;
}
DI void k2_issueB(K2Ctx& c, int slot) {
  const unsigned o = slot * K2_ST;
  glds_s(c.voffB, c.sB, c.ldsB + o); glds_s(c.voffB, c.sB + 128, c.ldsB + o + 8192);
  c.sB += 64 * 2048;
}
template <int SLOT> DI void k2_load(LAS char* lds, unsigned aA, const unsigned (&aB)[4], bf16x8 (&fb)[4], bf16x8 (&fa)[4]) {
#pragma unroll
  for (int m = 0; m < 4; ++m) fa[m] = *(LAS bf16x8*)(lds + aA + ((SLOT & 1) * K2_ST + m * 512));
#pragma unroll
  for (int i = 0; i < 4; ++i) fb[i] = tr_frag(lds + aB[i], (SLOT & 1) * K2_ST);
}
template <bool FIRST>
DI void k2_mma(f32x4 (&acc)[4][4], const bf16x8 (&fb)[4], const bf16x8 (&fa)[4]) {
  __builtin_amdgcn_s_setprio(1);
#pragma unroll
  for (int i = 0; i < 4; ++i)
#pragma unroll
    for (int m = 0; m < 4; ++m) { if (FIRST) MFMA_ACC0(acc[i][m], fb[i], fa[m]); else MFMA_ACC(acc[i][m], fb[i], fa[m]); }
  __builtin_amdgcn_s_setprio(0);
}
template <int SLOT, int ISSUE, int WAITN, bool LASTBAR, bool FIRST = false>
DI void k2_step(LAS char* lds, K2Ctx& c, const unsigned (&aA)[2], const unsigned (&aB)[2][4], f32x4 (&acc)[4][4]) {
  bf16x8 fb[4], fa[4];
  k2_load<SLOT>(lds, aA[SLOT >> 1], aB[SLOT >> 1], fb, fa);
  if (ISSUE) { k2_issueA(c, (SLOT + 3) & 3); k2_issueB(c, (SLOT + 3) & 3); }
  asm volatile("s_waitcnt vmcnt(%0)" ::"i"(WAITN) : "memory");
  WAIT_L0();
  BAR();
  k2_mma<FIRST>(acc, fb, fa);
  if (LASTBAR) BAR();
}
template <int ISSUE> DI void k2_step4(LAS char* lds, K2Ctx& c, const unsigned (&aA)[2], const unsigned (&aB)[2][4], f32x4 (&acc)[4][4]) {
  k2_step<0, ISSUE, 8, true>(lds, c, aA, aB, acc);
  k2_step<1, ISSUE, 8, true>(lds, c, aA, aB, acc);
  k2_step<2, ISSUE, 8, true>(lds, c, aA, aB, acc);
  k2_step<3, ISSUE, 8, true>(lds, c, aA, aB, acc);
}

constexpr int K1_A0 = 0, K1_ABUF = 32768, K1_B0 = 65536, K1_BBUF = 40960, K1_WPHI = 147456, K1_DUMP = 147456 + 2048, K1_LDS = 147456 + 2048 + 2048;

constexpr int k1_pos(int i) { return i < 8 ? (i >> 1) + 4 * (i & 1) : 17 - i; }
constexpr unsigned SW = F_ * 2, SQ = 3 * D_ * 2;

struct K1Ctx {
  const char* sA;
  const char* sG; const char* sU; const char* sQ;
  const char* vP2; const char* vP3;
  unsigned stepP3;
  unsigned voffA0, voffA1, voffW, voffQ;
  const char* pf;
  unsigned ldsD;
  const char* vPF;
  unsigned stepPF;
  unsigned ldsA, ldsB;
};

DI void k1_warm(K1Ctx& c) {
  asm volatile("s_mov_b32 m0, %1\n\ts_nop 0\n\tglobal_load_lds_dword %0, off" ::"v"(c.vPF), "s"(c.ldsD) : "memory");
  padd(c.vPF, c.stepPF);
}
template <int BUF, bool PF = true> DI void k1_issue_A(K1Ctx& c) {
#pragma unroll
  for (int i = 0; i < 4; ++i) glds_s((i & 1) ? c.voffA1 : c.voffA0, c.sA + i * 16384, c.ldsA + BUF * K1_ABUF + i * 1024);
  c.sA += 128;
  if (PF) k1_warm(c);
}
template <int BUF> DI void k1_issue_B(K1Ctx& c) {
  const unsigned l = c.ldsB + BUF * K1_BBUF;
  glds_s(c.voffW, c.sG, l);             c.sG += 64 * SW;
  glds_s(c.voffW, c.sU, l + 8192);      c.sU += 64 * SW;
  glds_v(c.vP2, l + 2 * 8192);          c.vP2 += 64 * SW;
  glds_v(c.vP3, l + 3 * 8192);          padd(c.vP3, c.stepP3);
  glds_s(c.voffQ, c.sQ, l + 4 * 8192);  c.sQ += 64 * SQ;
}

template <int BUF, int KS> DI void k1_load(LAS char* lds, const unsigned (&aB)[4], const unsigned (&aA)[2], bf16x8 (&fb)[10], bf16x8 (&fa)[4]) {
#pragma unroll
  for (int m = 0; m < 4; ++m) fa[m] = *(LAS bf16x8*)(lds + aA[KS] + (K1_A0 + BUF * K1_ABUF + m * 2048));
#pragma unroll
  for (int i = 0; i < 10; ++i) fb[i] = tr_frag(lds + aB[k1_pos(i) & 3], (k1_pos(i) >> 2) * 8192 + BUF * K1_BBUF + KS * 4096);
}
template <bool FIRST = false>
DI void k1_mma(f32x4 (&acc)[10][4], const bf16x8 (&fb)[10], const bf16x8 (&fa)[4]) {
  __builtin_amdgcn_s_setprio(1);
#pragma unroll
  for (int i = 0; i < 10; ++i)
#pragma unroll
    for (int m = 0; m < 4; ++m) { if (FIRST) MFMA_ACC0(acc[i][m], fb[i], fa[m]); else MFMA_ACC(acc[i][m], fb[i], fa[m]); }
  __builtin_amdgcn_s_setprio(0);
}
template <int GRP, int BUF, int MODE, bool FIRST = false>
DI void k1_tile(LAS char* lds, K1Ctx& c, const unsigned (&aB)[4], const unsigned (&aA)[2], f32x4 (&acc)[10][4]) {
  bf16x8 fb[10], fa[4];
  k1_load<BUF, 0>(lds, aB, aA, fb, fa);
  if (MODE < 2) { if (GRP == 0) k1_issue_A<BUF ^ 1>(c); else k1_issue_B<BUF ^ 1>(c); WAIT_V(10); }
  else {
    WAIT_V(0);
    if (c.pf) asm volatile("s_mov_b32 m0, %2\n\ts_nop 0\n\tglobal_load_lds_dword %0, %1" ::"v"(__builtin_amdgcn_mbcnt_hi(~0u, __builtin_amdgcn_mbcnt_lo(~0u, 0u)) * 128u), "s"(c.pf), "s"(c.ldsD) : "memory");
  }
  WAIT_L0();
  BAR();
  k1_mma<FIRST>(acc, fb, fa);
  BAR();
  k1_load<BUF, 1>(lds, aB, aA, fb, fa);
  if (MODE == 0) { if (GRP == 0) k1_issue_B<BUF>(c); else k1_issue_A<BUF>(c); WAIT_V(10); }
  else if (MODE == 1) { WAIT_V(5); }
  WAIT_L0();
  BAR();
  k1_mma(acc, fb, fa);
  if (!(MODE == 2 && GRP == 1)) BAR();
}
template <int GRP>
DI void k1_main(LAS char* lds, K1Ctx& c, const unsigned (&aB)[4], const unsigned (&aA)[2], f32x4 (&acc)[10][4]) {
  if (GRP == 1) BAR();
  k1_tile<GRP, 0, 0, true>(lds, c, aB, aA, acc);
  k1_tile<GRP, 1, 0>(lds, c, aB, aA, acc);
#pragma unroll 1
  for (int t = 2; t < 14; t += 2) {
    c.stepPF &= t <= 10 - GRP ? ~0u : 0u;
    k1_tile<GRP, 0, 0>(lds, c, aB, aA, acc);
    c.stepPF &= t + 1 <= 10 - GRP ? ~0u : 0u;
    k1_tile<GRP, 1, 0>(lds, c, aB, aA, acc);
  }
  k1_tile<GRP, 0, 1>(lds, c, aB, aA, acc);
  k1_tile<GRP, 1, 2>(lds, c, aB, aA, acc);
  MFMA_DRAIN();
}

DI void epi_h(const f32x4 (&ga)[4], const f32x4 (&ua)[4], char* pp) {
#pragma unroll
  for (int mp = 0; mp < 2; ++mp) {
    u32x2 w[2];
#pragma unroll
    for (int h = 0; h < 2; ++h) {
      const int m = 2 * mp + h;
      w[h][0] = gelu_mul2(ga[m].xy, ua[m].xy);
      w[h][1] = gelu_mul2(ga[m].zw, ua[m].zw);
    }
    st16_sc1(pp + mp * 1024, widen(w[0], w[1]));
  }
}
DI void load_wf(LAS char* wphi  , int lane, bf16x8 (&wf)[2]) {
  const int g = lane >> 4, q = (lane >> 2) & 3, p = lane & 3;
#pragma unroll
  for (int fb = 0; fb < 2; ++fb) {
    LAS char* a = wphi + (4 * g + q) * 64 + fb * 32 + p * 8;
    s16x4 lo = __builtin_amdgcn_ds_read_tr16_b64_v4i16((LAS s16x4*)a);
    s16x4 hi = __builtin_amdgcn_ds_read_tr16_b64_v4i16((LAS s16x4*)(a + 1024));
    wf[fb] = __builtin_shufflevector(lo, hi, 0, 1, 2, 3, 4, 5, 6, 7);
  }
}
DI void epi_kv(const bf16x8 (&kp)[4], const bf16x8 (&vp)[4], const bf16x8 (&wf)[2],
               LAS char* scr  , LAS char* red  , int lane) {
  const int g = lane >> 4, c = lane & 15, q = (lane >> 2) & 3, p = lane & 3;
  const u32x4 onesu = {0x3F803F80u, 0x3F803F80u, 0x3F803F80u, 0x3F803F80u};
  const bf16x8 ONES = __builtin_bit_cast(bf16x8, onesu);
  const f32x4 Z = {0.f, 0.f, 0.f, 0.f};
  LAS char* wr = scr + c * 200 + g * 8;
  LAS char* rd = scr + (8 * g + q) * 200 + p * 8;
  f32x4 kv[4][3];
#pragma unroll
  for (int a = 0; a < 4; ++a)
#pragma unroll
    for (int b = 0; b < 3; ++b) kv[a][b] = Z;
#pragma unroll
  for (int mp = 0; mp < 2; ++mp) {
    f32x4 pp[2][2];
#pragma unroll
    for (int h = 0; h < 2; ++h) { pp[h][0] = MFMA(wf[0], kp[2 * mp + h], Z); pp[h][1] = MFMA(wf[1], kp[2 * mp + h], Z); }
    bf16x8 fp[2], fn[2];
    feature_map2(pp, fp, fn);
#pragma unroll
    for (int h = 0; h < 2; ++h) {
      const u32x4 a = __builtin_bit_cast(u32x4, fp[h]), b = __builtin_bit_cast(u32x4, fn[h]), v = __builtin_bit_cast(u32x4, vp[2 * mp + h]);
      LAS char* w = wr + h * 3200;
      *(LAS u32x2*)(w) = a.xy;       *(LAS u32x2*)(w + 32) = a.zw;
      *(LAS u32x2*)(w + 64) = b.xy;  *(LAS u32x2*)(w + 96) = b.zw;
      *(LAS u32x2*)(w + 128) = v.xy; *(LAS u32x2*)(w + 160) = v.zw;
    }
    bf16x8 fr[6];
#pragma unroll
    for (int a = 0; a < 6; ++a) {
      s16x4 lo = __builtin_amdgcn_ds_read_tr16_b64_v4i16((LAS s16x4*)(rd + a * 32));
      s16x4 hi = __builtin_amdgcn_ds_read_tr16_b64_v4i16((LAS s16x4*)(rd + a * 32 + 800));
      fr[a] = __builtin_shufflevector(lo, hi, 0, 1, 2, 3, 4, 5, 6, 7);
    }
#pragma unroll
    for (int a = 0; a < 4; ++a) {
      kv[a][0] = MFMA(fr[a], fr[4], kv[a][0]);
      kv[a][1] = MFMA(fr[a], fr[5], kv[a][1]);
      kv[a][2] = MFMA(fr[a], ONES, kv[a][2]);
    }
  }
#pragma unroll
  for (int a = 0; a < 4; ++a) {
    *(LAS f32x4*)(red + (a * 2 + 0) * 1024 + lane * 16) = kv[a][0];
    *(LAS f32x4*)(red + (a * 2 + 1) * 1024 + lane * 16) = kv[a][1];
    if (c == 0) *(LAS f32x4*)(red + 8192 + a * 64 + g * 16) = kv[a][2];
  }
}

__global__ __launch_bounds__(512, 2) void fused_kernel(const unsigned short* __restrict__ x, const unsigned short* __restrict__ wqkv,
                                                       const unsigned short* __restrict__ wphi, const unsigned short* __restrict__ wg,
                                                       const unsigned short* __restrict__ wu, const unsigned short* __restrict__ gatew,
                                                       const char* __restrict__ wd, const char* __restrict__ wo,
                                                       char* act, char* kvp, float* gate, char* xbuf, unsigned* flags,
                                                       unsigned short* __restrict__ out) {
  unsigned long long pc_start;
  asm volatile("s_getpc_b64 %0" : "=s"(pc_start));
  extern __shared__ __attribute__((aligned(16))) char smem[];
  LAS char* lds = (LAS char*)smem;
  const unsigned ldsb = (unsigned)(size_t)lds;
  const int tid = threadIdx.x, lane = tid & 63;
  const int wid = __builtin_amdgcn_readfirstlane(tid >> 6);
  const int grp = wid >> 2, j = wid & 3, wm = 2 * grp + (j >> 1), wn = (j & 1) ^ grp;
  const int bid = blockIdx.x, xcd = bid & 7, idx = bid >> 3;
  const int rt = idx & 7, cg = xcd * 4 + (idx >> 3);
  const int row0 = rt * 256;
  const int g = lane >> 4, c = lane & 15;
  asm volatile("" ::"s"(x), "s"(wqkv), "s"(wphi), "s"(wg), "s"(wu), "s"(gatew), "s"(flags));
  unsigned epoch;
  asm volatile("global_load_dword %0, %1, %2 sc1" : "=&v"(epoch) : "v"(0u), "s"(flags) : "memory");
  u32x4 gx[4], gw[2];
  if (grp == 1) {
    const char* xr = (const char*)x + (size_t)(bid * 8 + 2 * j) * 2048;
    const unsigned vo = lane * 16;
    asm volatile("global_load_dwordx4 %0, %6, %7\n\tglobal_load_dwordx4 %1, %6, %7 offset:1024\n\t"
                 "global_load_dwordx4 %2, %6, %7 offset:2048\n\tglobal_load_dwordx4 %3, %6, %7 offset:3072\n\t"
                 "global_load_dwordx4 %4, %6, %8\n\tglobal_load_dwordx4 %5, %6, %8 offset:1024"
                 : "=&v"(gx[0]), "=&v"(gx[1]), "=&v"(gx[2]), "=&v"(gx[3]), "=&v"(gw[0]), "=&v"(gw[1]) : "v"(vo), "s"(xr), "s"(gatew) : "memory");
  }

  K1Ctx cx;
  {
    const int ar = (grp == 0 ? 128 : 0) + 32 * j;
    cx.sA = (const char*)x + (unsigned)(row0 + ar) * 2048u;
    cx.ldsA = ldsb + K1_A0 + ar * 128;
    const int r = lane >> 3, ch = lane & 7;
    cx.voffA0 = r * 2048 + ((ch ^ (r >> 1)) * 16);
    cx.voffA1 = cx.voffA0 ^ 64;
    const unsigned kr = grp * 32 + 8 * j;
    const int l32 = (ch >> 1) ^ (((r >> 1) & 1) | ((j & 1) << 1));
    const unsigned h16 = (ch & 1) * 16;
    cx.sG = (const char*)wg + kr * SW + cg * 224;
    cx.sU = (const char*)wu + kr * SW + cg * 224;
    cx.sQ = (const char*)wqkv + kr * SQ + cg * 64;
    cx.voffW = r * SW + l32 * 32 + h16;
    cx.voffQ = r * SQ + (l32 < 2 ? 4096 + l32 * 32 : (l32 - 2) * 32) + h16;
    cx.vP2 = (l32 ? cx.sG + 96 + l32 * 32 : cx.sU + 128) + (r * SW + h16);
    cx.vP3 = l32 < 2 ? cx.sU + (160 + l32 * 32 + r * SW + h16) : cx.sQ + (2048 + (l32 - 2) * 32 + r * SQ + h16);
    cx.stepP3 = l32 < 2 ? 64 * SW : 64 * SQ;
    cx.ldsB = ldsb + K1_B0 + kr * 128;
    cx.sA = uni_ptr(cx.sA); cx.sG = uni_ptr(cx.sG); cx.sU = uni_ptr(cx.sU); cx.sQ = uni_ptr(cx.sQ);
    asm volatile("s_nop 4" : "+s"(cx.sA), "+s"(cx.sG), "+s"(cx.sU), "+s"(cx.sQ));
  }
  if (wid < 2) glds_s(lane * 16, (const char*)wphi + cg * 2048 + wid * 1024, ldsb + K1_WPHI + wid * 1024);
  if (grp == 0) k1_issue_B<0>(cx); else k1_issue_A<0, false>(cx);
  BAR();
  if (grp == 0) k1_issue_A<0, false>(cx); else k1_issue_B<0>(cx);
  {
    cx.ldsD = ldsb + K1_DUMP + wid * 256;
    const int id = wid * 256 + bid;
    cx.pf = id < 896 ? wd + (size_t)id * 8192 : (id < 1152 ? wo + (size_t)(id - 896) * 8192 : nullptr);
    {
      const unsigned e = wid * 5 + (lane < 4 ? lane : 4);
      const unsigned k2 = e >= 20 ? 1u : 0u, ln = e - 20 * k2, krow = 192 + idx * 2 + k2;
      cx.vPF = e >= 40 ? (const char*)x + ((idx * 64 + e - 40) * 2048u + 384u)
             : ln < 7  ? (const char*)wg + (krow * SW + xcd * 896 + ln * 128)
             : ln < 14 ? (const char*)wu + (krow * SW + xcd * 896 + (ln - 7) * 128)
                       : (const char*)wqkv + (krow * SQ + xcd * 256 + ((ln - 14) >> 1) * 2048 + ((ln - 14) & 1) * 128);
      cx.stepPF = e >= 40 ? 128u : (ln < 14 ? 64 * SW : 64 * SQ);
    }
  }

  {
    const unsigned u = idx * 8 + wid;
    const char* pc0 = (const char*)(pc_start & ~(unsigned long long)0xff);
    const char* tp = pc0 + min(u * 3 + (lane < 3 ? lane : 0u), 519u) * 128u;
    if (lane == 3 && u < 41)
      tp = u < 28 ? (const char*)act + ((size_t)u << 20) : u < 32 ? (const char*)out + ((size_t)(u - 28) << 20)
         : u < 39 ? wd + ((size_t)(u - 32) << 20) : wo + ((size_t)(u - 39) << 20);
    asm volatile("s_mov_b32 m0, %1\n\ts_nop 0\n\tglobal_load_lds_dword %0, off" ::"v"(tp), "s"(cx.ldsD) : "memory");
  }

  unsigned aB[4], aA[2];
  {
    const int q = (lane >> 2) & 3, p = lane & 3;
    const int swz = ((q >> 1) & 1) | ((g & 1) << 1);
    const unsigned lb = K1_B0 + (8 * g + q) * 128 + p * 8;
#pragma unroll
    for (int i = 0; i < 4; ++i) aB[i] = lb + ((i ^ swz ^ (2 * wn)) * 32) + wn * (2 + (i >> 1)) * 8192;
    const int sa = (c >> 1) & 7;
    aA[0] = (64 * wm + c) * 128 + ((g ^ sa) * 16);
    aA[1] = aA[0] ^ 64;
  }

  f32x4 acc[10][4];

  asm volatile("s_waitcnt vmcnt(5)" : "+v"(epoch)::"memory");
  const unsigned tok = (unsigned)__builtin_amdgcn_readfirstlane(epoch) + 1u;
  BAR();
  if (grp == 0) k1_issue_B<1>(cx);
  else {
    k1_issue_A<1>(cx);
    asm volatile("" : "+v"(gx[0]), "+v"(gx[1]), "+v"(gx[2]), "+v"(gx[3]), "+v"(gw[0]), "+v"(gw[1]));
    float s0 = 0.f, s1 = 0.f;
#pragma unroll
    for (int h = 0; h < 2; ++h)
#pragma unroll
      for (int e = 0; e < 4; ++e) {
        const float wl = __uint_as_float(gw[h][e] << 16), wh = __uint_as_float(gw[h][e] & 0xffff0000u);
        s0 = __builtin_fmaf(__uint_as_float(gx[h][e] << 16), wl, s0);
        s0 = __builtin_fmaf(__uint_as_float(gx[h][e] & 0xffff0000u), wh, s0);
        s1 = __builtin_fmaf(__uint_as_float(gx[2 + h][e] << 16), wl, s1);
        s1 = __builtin_fmaf(__uint_as_float(gx[2 + h][e] & 0xffff0000u), wh, s1);
      }
    swap32(s0, s1);
    float sv = s0 + s1;
    { float t = sv; swap16(sv, t); sv += t; }
    sv += __uint_as_float(__builtin_amdgcn_update_dpp(0u, __float_as_uint(sv), 0x140, 0xf, 0xf, false));
    sv += __uint_as_float(__builtin_amdgcn_update_dpp(0u, __float_as_uint(sv), 0x141, 0xf, 0xf, false));
    sv += __uint_as_float(__builtin_amdgcn_update_dpp(0u, __float_as_uint(sv), 0x4e, 0xf, 0xf, false));
    sv += __uint_as_float(__builtin_amdgcn_update_dpp(0u, __float_as_uint(sv), 0xb1, 0xf, 0xf, false));
    const float gv = 1.0f / (1.0f + __expf(-sv));
    if ((lane & 31) == 0) {
      const float* gp = gate + bid * 8 + 2 * j;
      asm volatile("global_store_dword %0, %1, %2 sc1" ::"v"((unsigned)(lane >> 5) * 4u), "v"(gv), "s"(gp) : "memory");
    }
  }
  k1_warm(cx);
  if (grp == 0) k1_main<0>(lds, cx, aB, aA, acc); else k1_main<1>(lds, cx, aB, aA, acc);

  int tz = threadIdx.x;
  asm volatile("" : "+v"(tz));
  const int lane2 = tz & 63, g2 = lane2 >> 4;
  const int c2 = lane2 & 15, q2 = (lane2 >> 2) & 3, p2 = lane2 & 3;
  const unsigned lo = (unsigned)(row0 + 64 * wm + 16 * (g2 & 1) + c2) * 32u + (g2 >> 1) * 16u;
  char* hp = act + (size_t)(cg * 7 + 5 * wn) * PANEL + lo;
  if (wn != 0) __builtin_amdgcn_s_setprio(2);
  if (wn == 0) {
#pragma unroll
    for (int sl = 0; sl < 5; ++sl) { epi_h(acc[2 * sl], acc[2 * sl + 1], hp + (size_t)sl * PANEL); SCHED(); }
  }
  int wn2 = wn;
  asm volatile("" : "+s"(wn2));
  bf16x8 qp[4], wf[2];
  if (wn2 != 0) {
    epi_h(acc[0], acc[4], hp);
    SCHED();
    epi_h(acc[2], acc[6], hp + PANEL);
    SCHED();
    bf16x8 kp[4], vp[4];
#pragma unroll
    for (int m = 0; m < 4; ++m) {
      kp[m] = pack8(acc[1][m], acc[3][m]); vp[m] = pack8(acc[5][m], acc[7][m]); qp[m] = pack8(acc[9][m], acc[8][m]);
    }
    load_wf(lds + K1_WPHI, lane2, wf);
    epi_kv(kp, vp, wf, lds + 4 * 8448 + wm * 6400, lds + wm * 8448, lane2);
    __builtin_amdgcn_s_setprio(0);
  }
  WAIT_L0();
  BAR();
  for (int e = tz; e < 528; e += 512) {
    LAS char* p = lds + e * 16;
    const f32x4 sv = (*(LAS f32x4*)p + *(LAS f32x4*)(p + 8448)) + (*(LAS f32x4*)(p + 2 * 8448) + *(LAS f32x4*)(p + 3 * 8448));
    st8_sc1(kvp + ((size_t)rt * 32 + cg) * KVP + e * 8, pk4(sv));
  }
  WAIT_V(0);
  BAR();
  if (tz < 32) __hip_atomic_store(flags + FL_DONE + tz * 256 + bid, tok, RLX_AGENT);

  const int wm2 = j >> 1, wnn = j & 1;
  const int ks = xcd >> 2, qd = xcd & 3;
  const int tm = (qd >> 1) * 8 + (idx & 7), tn = (qd & 1) * 4 + (idx >> 3);
  K2Ctx cy;
  {
    const int r = lane2 >> 3, ch = lane2 & 7;
    const int kb = 2 * grp + (j >> 1);
    cy.sA = act + (size_t)(ks * 144 + kb) * PANEL + (tm * 128 + (j & 1) * 64) * 32;
    cy.ldsA = ldsb + kb * 4096 + (j & 1) * 2048;
    cy.vlin = lane2 * 16;
    const int kr = 32 * grp + 8 * j;
    cy.sB = wd + (size_t)(ks * 2304 + kr) * 2048 + tn * 256;
    cy.voffB = r * 2048 + (((ch >> 1) ^ (((r >> 1) & 1) | ((j & 1) << 1))) * 32) + (ch & 1) * 16;
    cy.ldsB = ldsb + 16384 + kr * 128;
  }
  k2_issueB(cy, 0); k2_issueB(cy, 1); k2_issueB(cy, 2); k2_issueB(cy, 3);
  const f32x4 Z = {0.f, 0.f, 0.f, 0.f};
  bf16x8 fp[4], fn[4];
  int wn3 = wn;
  asm volatile("" : "+s"(wn3));
  if (wn3 != 0) {
#pragma unroll
    for (int mp = 0; mp < 2; ++mp) {
      f32x4 pp[2][2];
#pragma unroll
      for (int h = 0; h < 2; ++h) { pp[h][0] = MFMA(wf[0], qp[2 * mp + h], Z); pp[h][1] = MFMA(wf[1], qp[2 * mp + h], Z); }
      bf16x8 a[2], b[2];
      feature_map2(pp, a, b);
      fp[2 * mp] = a[0]; fp[2 * mp + 1] = a[1]; fn[2 * mp] = b[0]; fn[2 * mp + 1] = b[1];
    }
  }
  LAS char* aux = lds + K2_AUX;
  if (wn3 == 0) {
    const unsigned* rep = flags + FL_DONE + (bid >> 3) * 256;
    wait_tok(rep + xcd + 8 * ((lane2 & 7) + (idx & 24)), tok);
    const char* kvb = kvp + cg * KVP;
    constexpr unsigned RS = 32u * KVP;
    const unsigned vo = (wm * 64 + lane2) * 16;
    u32x4 kq[8];
    asm volatile(LD8X4 : "=&v"(kq[0]), "=&v"(kq[1]), "=&v"(kq[2]), "=&v"(kq[3]), "=&v"(kq[4]), "=&v"(kq[5]), "=&v"(kq[6]), "=&v"(kq[7])
                 : "v"(vo), "s"(kvb), "s"(kvb + RS), "s"(kvb + 2 * RS), "s"(kvb + 3 * RS), "s"(kvb + 4 * RS), "s"(kvb + 5 * RS), "s"(kvb + 6 * RS), "s"(kvb + 7 * RS)
                 : "memory");
    *(LAS u32x4*)(aux + vo) = sum8bf(kq);
    if (wid == 2) {
      const unsigned vz = 4096 + (lane2 & 7) * 16;
      asm volatile(LD8X4 : "=&v"(kq[0]), "=&v"(kq[1]), "=&v"(kq[2]), "=&v"(kq[3]), "=&v"(kq[4]), "=&v"(kq[5]), "=&v"(kq[6]), "=&v"(kq[7])
                   : "v"(vz), "s"(kvb), "s"(kvb + RS), "s"(kvb + 2 * RS), "s"(kvb + 3 * RS), "s"(kvb + 4 * RS), "s"(kvb + 5 * RS), "s"(kvb + 6 * RS), "s"(kvb + 7 * RS)
                   : "memory");
      *(LAS u32x4*)(aux + vz) = sum8bf(kq);
    }
    if (wid == 0) {
      const unsigned* df = rep + lane2 * 4;
      for (unsigned spins = 0; spins < (1u << 17); ++spins) {
        u32x4 v;
        asm volatile("global_load_dwordx4 %0, %1, off sc1\n\ts_waitcnt vmcnt(0)" : "=&v"(v) : "v"(df) : "memory");
        if (__all(v[0] == tok && v[1] == tok && v[2] == tok && v[3] == tok)) break;
        __builtin_amdgcn_s_sleep(1);
      }
      if (bid == 0 && lane2 == 0) __hip_atomic_store(flags, tok, RLX_AGENT);
    }
  }
  WAIT_L0();
  BAR();
  float og[4];
  {
    {
      const unsigned vg = c2 * 4;
      const float* g1 = gate + tm * 128 + 64 * wm2;
      asm volatile("global_load_dword %0, %4, %5 sc1\n\tglobal_load_dword %1, %4, %5 offset:64 sc1\n\t"
                   "global_load_dword %2, %4, %5 offset:128 sc1\n\tglobal_load_dword %3, %4, %5 offset:192 sc1"
                   : "=&v"(og[0]), "=&v"(og[1]), "=&v"(og[2]), "=&v"(og[3]) : "v"(vg), "s"(g1) : "memory");
    }
    k2_issueA(cy, 0); k2_issueA(cy, 1); k2_issueA(cy, 2); k2_issueA(cy, 3);
    int wn4 = wn;
    asm volatile("" : "+s"(wn4));
    if (wn4 != 0) {
      bf16x8 kf[2][2], zf[2];
#pragma unroll
      for (int h = 0; h < 2; ++h) {
#pragma unroll
        for (int db = 0; db < 2; ++db) {
          const u32x2 l0 = *(LAS u32x2*)(aux + (((2 * h) * 2 + db) * 64 + lane2) * 8);
          const u32x2 h0 = *(LAS u32x2*)(aux + (((2 * h + 1) * 2 + db) * 64 + lane2) * 8);
          const u32x4 w = {l0[0], l0[1], h0[0], h0[1]};
          kf[h][db] = __builtin_bit_cast(bf16x8, w);
        }
        const u32x2 l0 = *(LAS u32x2*)(aux + (512 + (2 * h) * 4 + g2) * 8);
        const u32x2 h0 = *(LAS u32x2*)(aux + (512 + (2 * h + 1) * 4 + g2) * 8);
        const u32x4 w = {l0[0], l0[1], h0[0], h0[1]};
        zf[h] = __builtin_bit_cast(bf16x8, w);
      }
      char* op = act + (size_t)(224 + 2 * cg) * PANEL + lo;
#pragma unroll
      for (int mp = 0; mp < 2; ++mp) {
        u32x2 w0[2], w1[2];
#pragma unroll
        for (int h = 0; h < 2; ++h) {
          const int m = 2 * mp + h;
          f32x4 n0 = MFMA(kf[0][0], fp[m], Z); n0 = MFMA(kf[1][0], fn[m], n0);
          f32x4 n1 = MFMA(kf[0][1], fp[m], Z); n1 = MFMA(kf[1][1], fn[m], n1);
          f32x4 dn = MFMA(zf[0], fp[m], Z); dn = MFMA(zf[1], fn[m], dn);
          const float sc = 1.0f / (dn[0] + 1e-6f);
          w0[h] = pk4(n0 * sc); w1[h] = pk4(n1 * sc);
        }
        st16_sc1(op + mp * 1024, widen(w0[0], w0[1]));
        st16_sc1(op + PANEL + mp * 1024, widen(w1[0], w1[1]));
      }
    }
  }

  unsigned bA[2], bB[2][4];
  {
    bA[0] = (2 * grp + (g2 >> 1)) * 4096 + (64 * wm2 + c2) * 32 + (g2 & 1) * 16;
    bA[1] = bA[0] + 2 * K2_ST;
    const int swz = ((q2 >> 1) & 1) | ((g2 & 1) << 1);
#pragma unroll
    for (int i = 0; i < 4; ++i) {
      bB[0][i] = 16384 + wnn * 8192 + (32 * grp + 8 * g2 + q2) * 128 + p2 * 8 + ((i ^ swz) * 32);
      bB[1][i] = bB[0][i] + 2 * K2_ST;
    }
  }
  f32x4 ac2[4][4], ac3[4][4];

  asm volatile("s_waitcnt vmcnt(6)" : "+v"(og[0]), "+v"(og[1]), "+v"(og[2]), "+v"(og[3])::"memory");
  BAR();
  if (grp == 1) BAR();
  k2_step<0, 0, 4, true, true>(lds, cy, bA, bB, ac2);
  k2_step<1, 1, 6, true>(lds, cy, bA, bB, ac2);
  k2_step<2, 1, 8, true>(lds, cy, bA, bB, ac2);
  k2_step<3, 1, 8, true>(lds, cy, bA, bB, ac2);
  if (tz == 0) __hip_atomic_store(flags + FL_O + rt * 32 + cg, tok, RLX_AGENT);
#pragma unroll 1
  for (int t = 4; t < 12; t += 4) k2_step4<1>(lds, cy, bA, bB, ac2);
  const unsigned* ofl = flags + FL_O + (tm >> 1) * 32 + (lane2 & 31);
  unsigned seen;
  asm volatile("global_load_dword %0, %1, off sc1" : "=&v"(seen) : "v"(ofl) : "memory");
  k2_step4<1>(lds, cy, bA, bB, ac2);
  k2_step<0, 1, 8, false>(lds, cy, bA, bB, ac2);
  asm volatile("" : "+v"(seen));
  if (ks == 1) {
    if (wid == 0 && !__all(seen == tok)) wait_tok(ofl, tok);
    cy.sB = wo + (32 * grp + 8 * j) * 2048 + tn * 256;
  }
  BAR();
  k2_step<1, 1, 8, true>(lds, cy, bA, bB, ac2);
  k2_step<2, 1, 8, true>(lds, cy, bA, bB, ac2);
  k2_step<3, 1, 8, true>(lds, cy, bA, bB, ac2);
  k2_step<0, 1, 8, true, true>(lds, cy, bA, bB, ac3);
  k2_step<1, 1, 8, true>(lds, cy, bA, bB, ac3);
  k2_step<2, 1, 8, true>(lds, cy, bA, bB, ac3);
  k2_step<3, 1, 8, true>(lds, cy, bA, bB, ac3);
#pragma unroll 1
  for (int t = 24; t < 32; t += 4) k2_step4<1>(lds, cy, bA, bB, ac3);
  k2_step<0, 1, 8, true>(lds, cy, bA, bB, ac3);
  k2_step<1, 0, 4, true>(lds, cy, bA, bB, ac3);
  k2_step<2, 0, 0, true>(lds, cy, bA, bB, ac3);
  k2_step<3, 0, 0, false>(lds, cy, bA, bB, ac3);
  MFMA_DRAIN();
#pragma unroll
  for (int m = 0; m < 4; ++m) {
    const float fa = 1.0f - og[m], fb = ks ? og[m] : fa;
#pragma unroll
    for (int i = 0; i < 4; ++i) ac2[i][m] = ac2[i][m] * fa + ac3[i][m] * fb;
  }
  if (grp == 0) BAR();
  f32x4 mine[2][2];
  {
    LAS char* wr = lds + wid * 8192 + lane2 * 16;
    LAS char* rd = lds + (wid ^ 4) * 8192 + lane2 * 16;
    f32x4 snd[2][2];
#pragma unroll
    for (int i = 0; i < 2; ++i)
#pragma unroll
      for (int m = 0; m < 2; ++m) {
        const f32x4 l0 = sel4(ks, ac2[i][m], ac2[i][2 + m]), h0 = sel4(ks, ac2[2 + i][m], ac2[2 + i][2 + m]);
        *(LAS f32x4*)(wr + (i * 2 + m) * 1024) = sel4(grp, l0, h0);
        snd[i][m] = sel4(grp, h0, l0);
      }
#pragma unroll
    for (int i = 0; i < 2; ++i)
#pragma unroll
      for (int m = 0; m < 2; ++m) {
        const f32x4 l0 = sel4(ks, ac2[i][2 + m], ac2[i][m]), h0 = sel4(ks, ac2[2 + i][2 + m], ac2[2 + i][m]);
        *(LAS f32x4*)(wr + 4096 + (i * 2 + m) * 1024) = sel4(grp, l0, h0);
        mine[i][m] = sel4(grp, h0, l0);
      }
    WAIT_L0();
    BAR();
    char* xs = xbuf + ((size_t)bid * 8 + wid) * 4096 + lane2 * 16;
#pragma unroll
    for (int i = 0; i < 2; ++i)
#pragma unroll
      for (int m = 0; m < 2; ++m)
        st16_sc1(xs + (i * 2 + m) * 1024, __builtin_bit_cast(u32x4, snd[i][m] + *(LAS f32x4*)(rd + (i * 2 + m) * 1024)));
#pragma unroll
    for (int i = 0; i < 2; ++i)
#pragma unroll
      for (int m = 0; m < 2; ++m) mine[i][m] += *(LAS f32x4*)(rd + 4096 + (i * 2 + m) * 1024);
  }
  {
    WAIT_V(0);
    if (lane2 == 0) __hip_atomic_store(flags + FL_X + (bid * 8 + wid) * 16, tok, RLX_AGENT);
    wait_tok(flags + FL_X + ((bid ^ 4) * 8 + wid) * 16, tok);
    __builtin_amdgcn_fence(__ATOMIC_ACQUIRE, "wavefront");
    const char* xr = xbuf + ((size_t)(bid ^ 4) * 8 + wid) * 4096 + lane2 * 16;
    f32x4 o[4];
    asm volatile("global_load_dwordx4 %0, %4, off sc1\n\tglobal_load_dwordx4 %1, %4, off offset:1024 sc1\n\t"
                 "global_load_dwordx4 %2, %4, off offset:2048 sc1\n\tglobal_load_dwordx4 %3, %4, off offset:3072 sc1\n\t"
                 "s_waitcnt vmcnt(0)"
                 : "=&v"(o[0]), "=&v"(o[1]), "=&v"(o[2]), "=&v"(o[3]) : "v"(xr) : "memory");
#pragma unroll
    for (int m = 0; m < 2; ++m) {
      const f32x4 v0 = mine[0][m] + o[m];
      const f32x4 v1 = mine[1][m] + o[2 + m];
      const u32x4 w = widen(pk4(v0), pk4(v1));
      unsigned short* op = out + (size_t)(tm * 128 + 64 * wm2 + 32 * ks + 16 * m + c2) * D_ + tn * 128 + 64 * wnn + 32 * grp + 16 * (g2 & 1) + 8 * (g2 >> 1);
      *(u32x4*)op = w;
    }
  }
}

extern "C" void kernel_launch(void* const* d_in, const int* in_sizes, int n_in, void* d_out, int out_size, void* d_ws,
                              size_t ws_size, hipStream_t stream) {
  const unsigned short* x = (const unsigned short*)d_in[0];
  const unsigned short* wqkv = (const unsigned short*)d_in[1];
  const unsigned short* wphi = (const unsigned short*)d_in[2];
  const char* wo = (const char*)d_in[3];
  const unsigned short* wg = (const unsigned short*)d_in[5];
  const unsigned short* wu = (const unsigned short*)d_in[6];
  const char* wd = (const char*)d_in[7];
  const unsigned short* gatew = (const unsigned short*)d_in[8];
  char* ws = (char*)d_ws;
  char* act = ws;
  char* xbuf = ws + (18u << 20);
  char* kvp = ws + (26u << 20);
  unsigned* flags = (unsigned*)(ws + (26u << 20) + (1280u << 10));
  float* gate = (float*)(ws + (26u << 20) + (1536u << 10));
  (void)hipFuncSetAttribute((const void*)fused_kernel, hipFuncAttributeMaxDynamicSharedMemorySize, K1_LDS);
  fused_kernel<<<256, 512, K1_LDS, stream>>>(x, wqkv, wphi, wg, wu, gatew, wd, wo, act, kvp, gate, xbuf, flags, (unsigned short*)d_out);
}
```
